```python
import torch

def _softmax(x, axis=-1):
    return torch.softmax(x, dim=axis)
def _gelu(x):
    return torch.nn.functional.gelu(x, approximate="tanh")
def _silu(x):
    return torch.nn.functional.silu(x)
def _sigmoid(x):
    return torch.sigmoid(x)
def _layernorm(x, axis=-1, eps=1e-5):
    m = x.mean(dim=axis, keepdim=True)
    v = x.var(dim=axis, keepdim=True, unbiased=False)
    return (x - m) / torch.sqrt(v + eps)
def _scatter_add(tgt, idx, src, axis=0):
    return tgt.clone().index_add_(axis, idx, src)
def _segment_sum(src, seg, n):
    out = torch.zeros((n,) + src.shape[1:], dtype=src.dtype)
    return out.index_add_(0, seg, src)
def _pad(x, pw):
    flat = tuple(v for pair in reversed(pw) for v in pair)
    return torch.nn.functional.pad(x, flat)

kernel_name = 'gated_mix__gqa_block_cached__geglu_ffn__decode'

def setup_inputs():

    _w = (lambda a: (lambda t: t.to(torch.bfloat16) if t.is_floating_point() else t)(torch.as_tensor(a)))
    def _A_setup():
        B, Sq, D = 2, 1, 640
        Hq, Hkv, Sk, Dh = 32, 32, 8192, 128
        assert Hq % Hkv == 0, (Hq, Hkv)
        return {
            "x": _w((lambda *s: torch.randn(*s, dtype=torch.bfloat16) * 0.05)(B, Sq, D)),
            "k_cache": _w((lambda *s: torch.randn(*s, dtype=torch.bfloat16) * 6.4)(B, Hkv, Sk, Dh)),
            "v_cache": _w((lambda *s: torch.randn(*s, dtype=torch.bfloat16) * 0.2)(B, Hkv, Sk, Dh)),
            "wq": _w((lambda *s: torch.randn(*s, dtype=torch.bfloat16) * 6.4)(D, Hq * Dh) * (D ** -0.5)),
            "wo": _w((lambda *s: torch.randn(*s, dtype=torch.bfloat16) * 0.2)(Hq * Dh, D) * ((Hq * Dh) ** -0.5)),
        }
    _A_in = _A_setup()

    def _B_setup():
        B, S, D, F = 2, 1, 640, 1728
        return {
            "x": _w((lambda *s: torch.randn(*s, dtype=torch.bfloat16) * 0.05)(B, S, D)),
            "w_gate": _w((lambda *s: torch.randn(*s, dtype=torch.bfloat16) * 0.05)(D, F)),
            "w_up": _w((lambda *s: torch.randn(*s, dtype=torch.bfloat16) * 0.05)(D, F)),
            "w_down": _w((lambda *s: torch.randn(*s, dtype=torch.bfloat16) * 0.05)(F, D)),
        }
    _B_in = _B_setup()

    return {
        "A_x": _A_in["x"],
        "A_k_cache": _A_in["k_cache"],
        "A_v_cache": _A_in["v_cache"],
        "A_wq": _A_in["wq"],
        "A_wo": _A_in["wo"],
        "B_x": _B_in["x"],
        "B_w_gate": _B_in["w_gate"],
        "B_w_up": _B_in["w_up"],
        "B_w_down": _B_in["w_down"],
        "gate_w": _w((lambda *s: torch.randn(*s, dtype=torch.bfloat16) * 0.05)(640) * 16.0),
    }

def reference(A_x, A_k_cache, A_v_cache, A_wq, A_wo, B_x, B_w_gate, B_w_up, B_w_down, gate_w):
    def _A_ref(x, k_cache, v_cache, wq, wo):
        B, Sq, D = x.shape
        Hq, Dh = 32, 128
        Hkv = k_cache.shape[1]
        rep = Hq // Hkv
        q = (lambda x, *d: x.permute(*d))((x @ wq).reshape(B, Sq, Hq, Dh), 0, 2, 1, 3)
        k = (lambda x, n, axis: torch.repeat_interleave(x, n, dim=axis))(k_cache, rep, axis=1)
        v = (lambda x, n, axis: torch.repeat_interleave(x, n, dim=axis))(v_cache, rep, axis=1)
        scores = torch.einsum("bhqd,bhkd->bhqk", q, k) * (Dh ** -0.5)
        p = _softmax(scores, axis=-1)
        o = torch.einsum("bhqk,bhkd->bhqd", p, v)
        return (lambda x, *d: x.permute(*d))(o, 0, 2, 1, 3).reshape(B, Sq, Hq * Dh) @ wo

    def _B_ref(x, w_gate, w_up, w_down):
        return (_gelu(x @ w_gate) * (x @ w_up)) @ w_down

    x = A_x
    g = _sigmoid((x * gate_w).sum(-1, keepdims=True))
    a = _A_ref(x, A_k_cache, A_v_cache, A_wq, A_wo)
    b = _B_ref(x, B_w_gate, B_w_up, B_w_down)
    return g * a + (1 - g) * b
```

```cpp
#include <hip/hip_runtime.h>
#include <hip/hip_bf16.h>
#include <stdint.h>

typedef unsigned short u16;
typedef unsigned v4u __attribute__((ext_vector_type(4)));
typedef unsigned v2u __attribute__((ext_vector_type(2)));
typedef float f32x4 __attribute__((ext_vector_type(4)));
typedef __bf16 bf16x2 __attribute__((ext_vector_type(2)));
typedef __bf16 bf16x8 __attribute__((ext_vector_type(8)));
#define GAS __attribute__((address_space(1)))

constexpr int DM = 640, NH = 32, DH = 128, SK = 8192, FF = 1728;
constexpr int NSPLIT = 4, KEYS_WG = SK / NSPLIT, NWAVE = 8, NTHR = NWAVE * 64;
constexpr int WIN = 32;
constexpr int NGRP_F = FF / 64;

constexpr size_t WS_YA = 0;
constexpr size_t WS_YF = WS_YA + 256 * 640;
constexpr size_t WS_ML = WS_YF + 256 * 640;

constexpr int L_Q = 0;
constexpr int L_MW = L_Q + 8 * 128;
constexpr int L_LW = L_MW + 8;
constexpr int L_FP = L_LW + 8;
constexpr int L_O = L_FP + 8 * 16;
constexpr int L_Y = L_O + 128 * 8;
constexpr int L_END = L_Y + 8 * 640;

__device__ __forceinline__ float dot2(unsigned k, unsigned q, float acc) {
  return __builtin_amdgcn_fdot2_f32_bf16(__builtin_bit_cast(bf16x2, k), __builtin_bit_cast(bf16x2, q), acc, false);
}
__device__ __forceinline__ unsigned cvtpk(float lo, float hi) {
  bf16x2 r = {(__bf16)lo, (__bf16)hi};
  return __builtin_bit_cast(unsigned, r);
}
template <int CTRL> __device__ __forceinline__ float dpp(float x) {
  return __builtin_bit_cast(float, __builtin_amdgcn_mov_dpp(__builtin_bit_cast(int, x), CTRL, 0xf, 0xf, true));
}
template <int CTRL> __device__ __forceinline__ unsigned dppu(unsigned x) {
  return (unsigned)__builtin_amdgcn_mov_dpp((int)x, CTRL, 0xf, 0xf, true);
}
constexpr int XOR1 = 0xB1, XOR2 = 0x4E, XOR7 = 0x141, XOR8 = 0x128, ROR4 = 0x124, NEWBCAST = 0x150;
__device__ __forceinline__ float xrow16_max(float x) {
  auto s = __builtin_amdgcn_permlane16_swap(__float_as_uint(x), __float_as_uint(x), false, false);
  x = fmaxf(__uint_as_float(s[0]), __uint_as_float(s[1]));
  auto t = __builtin_amdgcn_permlane32_swap(__float_as_uint(x), __float_as_uint(x), false, false);
  return fmaxf(__uint_as_float(t[0]), __uint_as_float(t[1]));
}
__device__ __forceinline__ float xrow16_sum(float x) {
  auto s = __builtin_amdgcn_permlane16_swap(__float_as_uint(x), __float_as_uint(x), false, false);
  x = __uint_as_float(s[0]) + __uint_as_float(s[1]);
  auto t = __builtin_amdgcn_permlane32_swap(__float_as_uint(x), __float_as_uint(x), false, false);
  return __uint_as_float(t[0]) + __uint_as_float(t[1]);
}
__device__ __forceinline__ float wave_max(float x) {
  x = fmaxf(x, dpp<XOR1>(x)); x = fmaxf(x, dpp<XOR2>(x)); x = fmaxf(x, dpp<XOR7>(x)); x = fmaxf(x, dpp<XOR8>(x));
  return xrow16_max(x);
}
__device__ __forceinline__ float wave_sum(float x) {
  x += dpp<XOR1>(x); x += dpp<XOR2>(x); x += dpp<XOR7>(x); x += dpp<XOR8>(x);
  return xrow16_sum(x);
}
__device__ __forceinline__ f32x4 mfma(v4u a, v4u b, f32x4 c) {
  return __builtin_amdgcn_mfma_f32_16x16x32_bf16(__builtin_bit_cast(bf16x8, a), __builtin_bit_cast(bf16x8, b), c, 0, 0, 0);
}
__device__ __forceinline__ v4u ld_nt(const char* p) { return __builtin_nontemporal_load((const GAS v4u*)p); }
__device__ __forceinline__ v4u ld_pl(const char* p) { return *(const GAS v4u*)p; }
__device__ __forceinline__ float bf2f(unsigned lo16) { return __uint_as_float(lo16 << 16); }
__device__ __forceinline__ float bfround(float x) { return (float)(__bf16)x; }
__device__ __forceinline__ unsigned split_hilo(float p) {
  const unsigned t = cvtpk(p, p);
  const float hf = __uint_as_float(t & 0xffff0000u);
  return cvtpk(p, p - hf);
}
struct Sel { unsigned s[4]; };
__device__ __forceinline__ Sel make_sel(int j, bool pick_hi) {
  Sel r;
  const int e = j & 7;
  const unsigned two = pick_hi ? 0x0302u : 0x0100u;
  const unsigned v = (e & 1) ? ((two << 16) | 0x0c0cu) : (0x0c0c0000u | two);
#pragma unroll
  for (int d = 0; d < 4; ++d) r.s[d] = ((e >> 1) == d) ? v : 0x0c0c0c0cu;
  return r;
}
__device__ __forceinline__ v4u sel_frag(unsigned val, const Sel& s) {
  v4u b;
  b.x = __builtin_amdgcn_perm(val, val, s.s[0]);
  b.y = __builtin_amdgcn_perm(val, val, s.s[1]);
  b.z = __builtin_amdgcn_perm(val, val, s.s[2]);
  b.w = __builtin_amdgcn_perm(val, val, s.s[3]);
  return b;
}
template <int T> __device__ __forceinline__ unsigned bcast16(unsigned x) { return dppu<NEWBCAST + T>(x); }

#define SB() __builtin_amdgcn_sched_barrier(0)

template <int I> struct IC { static constexpr int v = I; };
template <int N, int I = 0, class Fn> __device__ __forceinline__ void static_for(Fn&& f) {
  if constexpr (I < N) { f(IC<I>{}); static_for<N, I + 1>(f); }
}

__global__ void __launch_bounds__(NTHR, 2)
attn_main(const u16* __restrict__ x, const u16* __restrict__ kc, const u16* __restrict__ vc, const u16* __restrict__ wq,
          const u16* __restrict__ wo, const u16* __restrict__ wg, const u16* __restrict__ wu, const u16* __restrict__ wd,
          float* __restrict__ ws) {
  __shared__ __attribute__((aligned(16))) float lds[L_END];
  const int tid = threadIdx.x, lane = tid & 63, w = __builtin_amdgcn_readfirstlane(tid >> 6);
  const int bid = blockIdx.x, jj = bid >> 3, h = (bid & 7) + 8 * (jj >> 3), sub = jj & 7, b = sub & 1, s = sub >> 1;
  const int g = lane >> 4, c = lane & 15;

  unsigned X[10];
  {
    const GAS v2u* xp = (const GAS v2u*)((const char*)x + b * 1280 + 160 * w + 40 * g);
#pragma unroll
    for (int i = 0; i < 5; ++i) { v2u t = xp[i]; X[2 * i] = t.x; X[2 * i + 1] = t.y; }
  }
  v4u WQ[20];
  {
    const char* base = (const char*)wq + (size_t)(80 * w) * 8192 + h * 256;
    const unsigned voff = (unsigned)(20 * g) * 8192u + c * 16;
#pragma unroll
    for (int i = 0; i < 20; ++i) WQ[i] = ld_pl(base + i * 8192 + voff);
  }
  SB();
  const size_t kvoff = ((size_t)(b * NH + h) * SK + s * KEYS_WG + w * (KEYS_WG / NWAVE)) * 256;
  const char* kbase = (const char*)kc + kvoff;
  const char* vbase = (const char*)vc + kvoff;
  const unsigned loff = lane * 16;
  v4u win[WIN];
#pragma unroll
  for (int i = 0; i < 16; ++i) win[i] = ld_nt(kbase + i * 1024 + loff);
  SB();
  const bool has_ffn = h < NGRP_F;
  const int n0 = 64 * h + 16 * s;
  v4u FW[5];
  unsigned FX[5];
  if (has_ffn) {
    const char* fbase = (const char*)(w < 4 ? wg : wu) + (size_t)(160 * (w & 3)) * (FF * 2) + n0 * 2;
    const unsigned fvoff = (unsigned)(lane >> 1) * (FF * 2) + (lane & 1) * 16;
#pragma unroll
    for (int k = 0; k < 5; ++k) FW[k] = ld_pl(fbase + (size_t)k * 32 * (FF * 2) + fvoff);
    const GAS u16* xr = (const GAS u16*)((const char*)x + b * 1280) + 160 * (w & 3) + (lane >> 1);
#pragma unroll
    for (int k = 0; k < 5; ++k) FX[k] = xr[32 * k];
  } else {
#pragma unroll
    for (int k = 0; k < 5; ++k) { FW[k] = v4u{0, 0, 0, 0}; FX[k] = 0; }
  }
  SB();

  const Sel selLo = make_sel(c, false), selHi = make_sel(c, true);
  {
    f32x4 qa = {0.f, 0.f, 0.f, 0.f};
#pragma unroll
    for (int i = 0; i < 20; ++i) qa = mfma(WQ[i], sel_frag(X[i >> 1], (i & 1) ? selHi : selLo), qa);
    if (c < 8) {
#pragma unroll
      for (int r = 0; r < 4; ++r) lds[L_Q + w * 128 + 32 * g + 8 * r + c] = qa[r];
    }
  }
  SB();
#pragma unroll
  for (int i = 16; i < WIN; ++i) win[i] = ld_nt(kbase + i * 1024 + loff);
  SB();
  __syncthreads();
  unsigned q0, q1, q2, q3;
  {
    float qv[8];
    const f32x4* p0 = (const f32x4*)&lds[L_Q + (2 * g) * 128 + 8 * c];
    const f32x4* p1 = (const f32x4*)&lds[L_Q + (2 * g + 1) * 128 + 8 * c];
    const f32x4 a0 = p0[0], a1 = p0[1], b0 = p1[0], b1 = p1[1];
#pragma unroll
    for (int e = 0; e < 4; ++e) { qv[e] = xrow16_sum(a0[e] + b0[e]); qv[4 + e] = xrow16_sum(a1[e] + b1[e]); }
    q0 = cvtpk(qv[0], qv[1]); q1 = cvtpk(qv[2], qv[3]); q2 = cvtpk(qv[4], qv[5]); q3 = cvtpk(qv[6], qv[7]);
  }

  {
    float fa[8];
#pragma unroll
    for (int e = 0; e < 8; ++e) fa[e] = 0.f;
#pragma unroll
    for (int k = 0; k < 5; ++k) {
      const float xf = bf2f(FX[k]);
#pragma unroll
      for (int d = 0; d < 4; ++d) {
        fa[2 * d] = fmaf(xf, __uint_as_float(FW[k][d] << 16), fa[2 * d]);
        fa[2 * d + 1] = fmaf(xf, __uint_as_float(FW[k][d] & 0xffff0000u), fa[2 * d + 1]);
      }
    }
#pragma unroll
    for (int e = 0; e < 8; ++e) {
      float t = fa[e];
      t += dpp<XOR2>(t); t += dpp<ROR4>(t); t += dpp<XOR8>(t);
      fa[e] = xrow16_sum(t);
    }
    if (lane < 2) {
#pragma unroll
      for (int e = 0; e < 8; ++e) lds[L_FP + w * 16 + 8 * lane + e] = fa[e];
    }
  }

  float sc[4];
  {
    float xs[16];
    static_for<64>([&](auto I) {
      constexpr int i = I.v;
      const v4u cur = win[i % WIN];
      float d = dot2(cur.x, q0, 0.f);
      d = dot2(cur.y, q1, d); d = dot2(cur.z, q2, d); d = dot2(cur.w, q3, d);
      xs[i % 16] = d;
      constexpr int n = i + WIN;
      if constexpr (n < 64) win[i % WIN] = ld_nt(kbase + n * 1024 + loff);
      else win[i % WIN] = ld_nt(vbase + (n - 64) * 1024 + loff);
      if constexpr (i % 16 == 15) {
#define TR_STEP(HALF, CTRL, BIT) _Pragma("unroll") for (int hh = 0; hh < (HALF); ++hh) { \
      const float lo_ = xs[hh], hi_ = xs[hh + (HALF)]; const float t1_ = lo_ + dpp<CTRL>(lo_), t2_ = hi_ + dpp<CTRL>(hi_); \
      xs[hh] = (c & (BIT)) ? t2_ : t1_; }
        TR_STEP(8, XOR8, 8) TR_STEP(4, XOR7, 4) TR_STEP(2, XOR2, 2) TR_STEP(1, XOR1, 1)
#undef TR_STEP
        sc[i / 16] = xs[0];
      }
      SB();
    });
  }
  constexpr float C2 = 0.08838834764831845f * 1.4426950408889634f;
  {
    float m = fmaxf(fmaxf(sc[0], sc[1]), fmaxf(sc[2], sc[3]));
    m = wave_max(m);
    if (lane == 0) lds[L_MW + w] = m;
  }
  __syncthreads();
  float mg;
  {
    const f32x4 m0 = *(const f32x4*)&lds[L_MW], m1 = *(const f32x4*)&lds[L_MW + 4];
    mg = fmaxf(fmaxf(fmaxf(m0[0], m0[1]), fmaxf(m0[2], m0[3])), fmaxf(fmaxf(m1[0], m1[1]), fmaxf(m1[2], m1[3])));
  }
  const float mC = mg * C2;
  unsigned PP[4];
  {
    float l = 0.f;
#pragma unroll
    for (int t = 0; t < 4; ++t) {
      const float p = __builtin_amdgcn_exp2f(fmaf(sc[t], C2, -mC));
      l += p;
      PP[t] = split_hilo(p);
    }
    l = wave_sum(l);
    if (lane == 0) lds[L_LW + w] = l;
  }
  const Sel selM = make_sel(c, c >= 8);
  f32x4 yf = {0.f, 0.f, 0.f, 0.f};
  if (has_ffn && w < 5) {
    const char* dbase = (const char*)wd + (size_t)n0 * 1280 + w * 256;
    const unsigned dvoff = g * 1280 + c * 16;
    v4u DW[4];
#pragma unroll
    for (int i = 0; i < 4; ++i) DW[i] = ld_pl(dbase + i * 5120 + dvoff);
    const float gt = bfround(lds[L_FP + 0 * 16 + c] + lds[L_FP + 1 * 16 + c] + lds[L_FP + 2 * 16 + c] + lds[L_FP + 3 * 16 + c]);
    const float up = bfround(lds[L_FP + 4 * 16 + c] + lds[L_FP + 5 * 16 + c] + lds[L_FP + 6 * 16 + c] + lds[L_FP + 7 * 16 + c]);
    const float u = 0.7978845608028654f * (gt + 0.044715f * gt * gt * gt);
    const float th = 1.f - 2.f / (1.f + __expf(2.f * u));
    const float ge = bfround(0.5f * gt * (1.f + th));
    const unsigned hb = cvtpk(ge * up, 0.f);
#pragma unroll
    for (int i = 0; i < 4; ++i) {
      const unsigned hv = (unsigned)__builtin_amdgcn_ds_bpermute((4 * i + g) * 4, (int)hb);
      yf = mfma(DW[i], sel_frag(hv, selLo), yf);
    }
  }

  f32x4 acc = {0.f, 0.f, 0.f, 0.f};
  const char* obase = (const char*)wo + (size_t)(h * 128 + 16 * w) * 1280;
  const unsigned ovoff = g * 1280 + c * 16;
  static_for<64>([&](auto I) {
    constexpr int i = I.v;
    const v4u cur = win[(64 + i) % WIN];
    const unsigned pb = bcast16<i % 16>(PP[i / 16]);
    acc = mfma(cur, sel_frag(pb, selM), acc);
    constexpr int n = i + WIN;
    if constexpr (n < 64) win[(64 + i) % WIN] = ld_nt(vbase + n * 1024 + loff);
    else if constexpr (n - 64 < 20) win[(64 + i) % WIN] = ld_pl(obase + ((n - 64) / 5) * 5120 + ((n - 64) % 5) * 256 + ovoff);
    SB();
  });
#pragma unroll
  for (int r = 0; r < 4; ++r) acc[r] += dpp<XOR8>(acc[r]);
  if (c < 8) {
#pragma unroll
    for (int r = 0; r < 4; ++r) lds[L_O + (32 * g + 8 * r + c) * 8 + w] = acc[r];
  }
  __syncthreads();
  f32x4 ya[5];
#pragma unroll
  for (int k = 0; k < 5; ++k) ya[k] = f32x4{0.f, 0.f, 0.f, 0.f};
#pragma unroll
  for (int i = 0; i < 4; ++i) {
    const f32x4* op = (const f32x4*)&lds[L_O + (16 * w + 4 * i + g) * 8];
    const f32x4 a = op[0], bb = op[1];
    const float o = ((a[0] + a[1]) + (a[2] + a[3])) + ((bb[0] + bb[1]) + (bb[2] + bb[3]));
    const v4u bf = sel_frag(split_hilo(o), selM);
#pragma unroll
    for (int k = 0; k < 5; ++k) ya[k] = mfma(win[(64 + 32 + 5 * i + k) % WIN], bf, ya[k]);
  }
#pragma unroll
  for (int k = 0; k < 5; ++k)
#pragma unroll
    for (int r = 0; r < 4; ++r) {
      const float t = ya[k][r] + dpp<XOR8>(ya[k][r]);
      if (c < 8) lds[L_Y + w * 640 + 128 * k + 32 * g + 8 * r + c] = t;
    }
  if (w < 5 && c < 8) {
    float* yfp = ws + WS_YF + (size_t)bid * 640 + 128 * w + 32 * g + c;
#pragma unroll
    for (int r = 0; r < 4; ++r) yfp[8 * r] = yf[r];
  }
  __syncthreads();
  if (tid < 160) {
    f32x4 t = *(const f32x4*)&lds[L_Y + 4 * tid];
#pragma unroll
    for (int ww = 1; ww < 8; ++ww) t += *(const f32x4*)&lds[L_Y + ww * 640 + 4 * tid];
    *(f32x4*)(ws + WS_YA + (size_t)bid * 640 + 4 * tid) = t;
  }
  if (tid == 192) {
    const f32x4 l0 = *(const f32x4*)&lds[L_LW], l1 = *(const f32x4*)&lds[L_LW + 4];
    const float l = ((l0[0] + l0[1]) + (l0[2] + l0[3])) + ((l1[0] + l1[1]) + (l1[2] + l1[3]));
    ws[WS_ML + 2 * bid] = mC;
    ws[WS_ML + 2 * bid + 1] = l;
  }
}

__global__ void __launch_bounds__(256)
final_reduce(const u16* __restrict__ x, const u16* __restrict__ gw, const float* __restrict__ ws, u16* __restrict__ out) {
  __shared__ float cw[128];
  __shared__ float red[16][64];
  __shared__ float redf[16][64];
  __shared__ float gsum[4];
  const int b = blockIdx.x / 10, cb = blockIdx.x % 10, tid = threadIdx.x;
  if (tid < 128) {
    const int h = tid >> 2, s = tid & 3;
    float mm[4], ll[4];
#pragma unroll
    for (int k = 0; k < 4; ++k) {
      const int bid = (h & 7) + 8 * ((h >> 3) * 8 + k * 2 + b);
      mm[k] = ws[WS_ML + 2 * bid]; ll[k] = ws[WS_ML + 2 * bid + 1];
    }
    const float M = fmaxf(fmaxf(mm[0], mm[1]), fmaxf(mm[2], mm[3]));
    float L = 0.f;
#pragma unroll
    for (int k = 0; k < 4; ++k) L += ll[k] * exp2f(mm[k] - M);
    cw[tid] = exp2f(mm[s] - M) / L;
  }
  {
    float p = 0.f;
    for (int i = tid; i < 640; i += 256)
      p += bfround(bf2f(x[b * 640 + i]) * bf2f(gw[i]));
    p = wave_sum(p);
    if ((tid & 63) == 0) gsum[tid >> 6] = p;
  }
  __syncthreads();
  const int rg = tid >> 4, cq = tid & 15;
  f32x4 a = {0.f, 0.f, 0.f, 0.f}, f = {0.f, 0.f, 0.f, 0.f};
#pragma unroll
  for (int k = 0; k < 8; ++k) {
    const int e = rg * 8 + k, h = e >> 2, s = e & 3;
    const int bid = (h & 7) + 8 * ((h >> 3) * 8 + s * 2 + b);
    const f32x4 t = *(const f32x4*)(ws + WS_YA + (size_t)bid * 640 + cb * 64 + cq * 4);
    a += cw[e] * t;
    if (h < NGRP_F) f += *(const f32x4*)(ws + WS_YF + (size_t)bid * 640 + cb * 64 + cq * 4);
  }
#pragma unroll
  for (int e = 0; e < 4; ++e) { red[rg][cq * 4 + e] = a[e]; redf[rg][cq * 4 + e] = f[e]; }
  __syncthreads();
  if (tid < 64) {
    float sa = 0.f, sf = 0.f;
#pragma unroll
    for (int k = 0; k < 16; ++k) { sa += red[k][tid]; sf += redf[k][tid]; }
    const float z = bfround(gsum[0] + gsum[1] + gsum[2] + gsum[3]);
    const float gt = bfround(1.f / (1.f + __expf(-z)));
    const float omg = bfround(1.f - gt);
    const float r = bfround(gt * bfround(sa)) + bfround(omg * bfround(sf));
    out[b * 640 + cb * 64 + tid] = (u16)(cvtpk(r, 0.f) & 0xffffu);
  }
}

extern "C" void kernel_launch(void* const* d_in, const int* in_sizes, int n_in, void* d_out, int out_size, void* d_ws,
                              size_t ws_size, hipStream_t stream) {
  const u16* x = (const u16*)d_in[0];
  const u16* kc = (const u16*)d_in[1];
  const u16* vc = (const u16*)d_in[2];
  const u16* wq = (const u16*)d_in[3];
  const u16* wo = (const u16*)d_in[4];
  const u16* wg = (const u16*)d_in[6];
  const u16* wu = (const u16*)d_in[7];
  const u16* wd = (const u16*)d_in[8];
  const u16* gw = (const u16*)d_in[9];
  float* ws = (float*)d_ws;
  attn_main<<<256, NTHR, 0, stream>>>(x, kc, vc, wq, wo, wg, wu, wd, ws);
  final_reduce<<<20, 256, 0, stream>>>(x, gw, ws, (u16*)d_out);
}
```

```cpp
#include <hip/hip_runtime.h>
#include <hip/hip_bf16.h>
#include <stdint.h>

typedef unsigned short u16;
typedef unsigned v4u __attribute__((ext_vector_type(4)));
typedef unsigned v2u __attribute__((ext_vector_type(2)));
typedef float f32x4 __attribute__((ext_vector_type(4)));
typedef __bf16 bf16x2 __attribute__((ext_vector_type(2)));
typedef __bf16 bf16x8 __attribute__((ext_vector_type(8)));
#define GAS __attribute__((address_space(1)))
#define LAS __attribute__((address_space(3)))
typedef __amdgpu_buffer_rsrc_t rsrc_t;

constexpr int DM = 640, NH = 32, DH = 128, SK = 8192, FF = 1728;
constexpr int NSPLIT = 4, KEYS_WG = SK / NSPLIT, NWAVE = 8, NTHR = NWAVE * 64;
#define WIN 4
#define PF 12
#define POLL_AT (PF - 4)
#define OWK0 8
#define OWSTR 5
#define OWAUX 2
#define WNT 2
constexpr int NGRP_F = FF / 64;
constexpr int AUX_NT = 2, AUX_SC1 = 16;
constexpr int NRED = 40;
constexpr unsigned SENT = 0xFFFFFFFFu;
constexpr unsigned OOB = 0x40000000u;
constexpr unsigned SPIN_CAP = 1u << 18;

constexpr int WS_EPOCH = 0;
constexpr int WS_G = 1024;
constexpr int WS_P = 4096;
constexpr int WS_Y = WS_P + 256 * 4096;
constexpr int WS_BYTES = WS_Y + 256 * 640 * 4;

constexpr int L_P = 0;
constexpr int L_Q = L_P + 8 * 256;
constexpr int L_MW = L_Q + 8 * 128;
constexpr int L_LW = L_MW + 8;
constexpr int L_FP = L_LW + 8;
constexpr int L_GP = L_FP + 8 * 32;
constexpr int L_CNT = L_GP + 8;
constexpr int L_O = L_CNT + 8;
constexpr int L_Y = L_O + 128 * 8;
constexpr int L_R = L_Y + 9 * 640;
constexpr int L_END = L_R + 8 * 32;
constexpr int LDS_BYTES = 96 * 1024;
static_assert(L_END * 4 <= LDS_BYTES, "lds");

__device__ __forceinline__ float dot2(unsigned k, unsigned q, float acc) {
  return __builtin_amdgcn_fdot2_f32_bf16(__builtin_bit_cast(bf16x2, k), __builtin_bit_cast(bf16x2, q), acc, false);
}
__device__ __forceinline__ unsigned cvtpk(float lo, float hi) {
  bf16x2 r = {(__bf16)lo, (__bf16)hi};
  return __builtin_bit_cast(unsigned, r);
}
template <int CTRL> __device__ __forceinline__ float dpp(float x) {
  return __builtin_bit_cast(float, __builtin_amdgcn_mov_dpp(__builtin_bit_cast(int, x), CTRL, 0xf, 0xf, true));
}
template <int CTRL> __device__ __forceinline__ unsigned dppu(unsigned x) {
  return (unsigned)__builtin_amdgcn_mov_dpp((int)x, CTRL, 0xf, 0xf, true);
}
constexpr int XOR1 = 0xB1, XOR2 = 0x4E, XOR7 = 0x141, XOR8 = 0x128, ROR4 = 0x124, NEWBCAST = 0x150;
__device__ __forceinline__ float xrow16_max(float x) {
  auto s = __builtin_amdgcn_permlane16_swap(__float_as_uint(x), __float_as_uint(x), false, false);
  x = fmaxf(__uint_as_float(s[0]), __uint_as_float(s[1]));
  auto t = __builtin_amdgcn_permlane32_swap(__float_as_uint(x), __float_as_uint(x), false, false);
  return fmaxf(__uint_as_float(t[0]), __uint_as_float(t[1]));
}
__device__ __forceinline__ float xrow16_sum(float x) {
  auto s = __builtin_amdgcn_permlane16_swap(__float_as_uint(x), __float_as_uint(x), false, false);
  x = __uint_as_float(s[0]) + __uint_as_float(s[1]);
  auto t = __builtin_amdgcn_permlane32_swap(__float_as_uint(x), __float_as_uint(x), false, false);
  return __uint_as_float(t[0]) + __uint_as_float(t[1]);
}
__device__ __forceinline__ float wave_max(float x) {
  x = fmaxf(x, dpp<XOR1>(x)); x = fmaxf(x, dpp<XOR2>(x)); x = fmaxf(x, dpp<XOR7>(x)); x = fmaxf(x, dpp<XOR8>(x));
  return xrow16_max(x);
}
__device__ __forceinline__ float wave_sum(float x) {
  x += dpp<XOR1>(x); x += dpp<XOR2>(x); x += dpp<XOR7>(x); x += dpp<XOR8>(x);
  return xrow16_sum(x);
}
__device__ __forceinline__ f32x4 mfma(v4u a, v4u b, f32x4 c) {
  return __builtin_amdgcn_mfma_f32_16x16x32_bf16(__builtin_bit_cast(bf16x8, a), __builtin_bit_cast(bf16x8, b), c, 0, 0, 0);
}
__device__ __forceinline__ rsrc_t mk_rsrc(const void* p, unsigned bytes) {
  return __builtin_amdgcn_make_buffer_rsrc((void*)p, 0, bytes, 0x00020000);
}
template <int AUX> __device__ __forceinline__ v4u bld(rsrc_t r, unsigned voff, unsigned soff) {
  return __builtin_amdgcn_raw_buffer_load_b128(r, voff, soff, AUX);
}
__device__ __forceinline__ float bf2f(unsigned lo16) { return __uint_as_float(lo16 << 16); }
__device__ __forceinline__ float bfround(float x) { return (float)(__bf16)x; }
__device__ __forceinline__ unsigned split_hilo(float p) {
  const unsigned t = cvtpk(p, p);
  const float hf = __uint_as_float(t & 0xffff0000u);
  return cvtpk(p, p - hf);
}
template <int CTRL> __device__ __forceinline__ float merge2(float a, float b, bool pick_b) {
  const float ta = a + dpp<CTRL>(a), tb = b + dpp<CTRL>(b);
  return pick_b ? tb : ta;
}
struct Sel { unsigned s[4]; };
__device__ __forceinline__ Sel make_sel(int j, bool pick_hi) {
  Sel r;
  const int e = j & 7;
  const unsigned two = pick_hi ? 0x0302u : 0x0100u;
  const unsigned v = (e & 1) ? ((two << 16) | 0x0c0cu) : (0x0c0c0000u | two);
#pragma unroll
  for (int d = 0; d < 4; ++d) r.s[d] = ((e >> 1) == d) ? v : 0x0c0c0c0cu;
  return r;
}
__device__ __forceinline__ v4u sel_frag(unsigned val, const Sel& s) {
  v4u b;
  b.x = __builtin_amdgcn_perm(val, val, s.s[0]);
  b.y = __builtin_amdgcn_perm(val, val, s.s[1]);
  b.z = __builtin_amdgcn_perm(val, val, s.s[2]);
  b.w = __builtin_amdgcn_perm(val, val, s.s[3]);
  return b;
}
template <int T> __device__ __forceinline__ unsigned bcast16(unsigned x) { return dppu<NEWBCAST + T>(x); }

#define SB() __builtin_amdgcn_sched_barrier(0)
#define MEMFENCE() asm volatile("" ::: "memory")
#define LGKM0() asm volatile("s_waitcnt lgkmcnt(0)" ::: "memory")

template <int I> struct IC { static constexpr int v = I; };
template <int N, int I = 0, class Fn> __device__ __forceinline__ void static_for(Fn&& f) {
  if constexpr (I < N) { f(IC<I>{}); static_for<N, I + 1>(f); }
}
#define STAMP(k) do {} while (0)

__global__ void __launch_bounds__(NTHR, 2)
fused_decode(const u16* __restrict__ x, const u16* __restrict__ kc, const u16* __restrict__ vc, const u16* __restrict__ wq,
             const u16* __restrict__ wo, const u16* __restrict__ wg, const u16* __restrict__ wu, const u16* __restrict__ wd,
             const u16* __restrict__ gw, u16* __restrict__ out, unsigned char* __restrict__ ws) {
  extern __shared__ __attribute__((aligned(16))) float lds[];
  const int tid = threadIdx.x, lane = tid & 63, w = __builtin_amdgcn_readfirstlane(tid >> 6);
  const int bid = blockIdx.x, jj = bid >> 3, h = (bid & 7) + 8 * (jj >> 3), sub = jj & 7, b = sub & 1, s = sub >> 1;
  const int g = lane >> 4, c = lane & 15;
  STAMP(0);
  const rsrc_t rsW = mk_rsrc(ws, WS_BYTES);
  const bool has_ffn = h < NGRP_F;
  const int r0 = 80 * sub + 20 * (w & 3) + g;
  const GAS u16* xp = (const GAS u16*)x + (c >> 3) * DM + r0;
  unsigned XB[5];
  v4u WS5[5];
  {
    const char* wp = (w < 4) ? (const char*)wq + (size_t)r0 * 8192 + h * 256 + c * 16
                             : (const char*)(c < 8 ? wg : wu) + (size_t)r0 * (FF * 2) + (has_ffn ? h : NGRP_F - 1) * 128 + (c & 7) * 16;
    const unsigned rstr = (w < 4) ? 4u * 8192u : 4u * (FF * 2);
#pragma unroll
    for (int i = 0; i < 5; ++i) WS5[i] = __builtin_nontemporal_load((const GAS v4u*)(wp + (size_t)i * rstr));
#pragma unroll
    for (int i = 0; i < 5; ++i) XB[i] = xp[4 * i];
  }
  const unsigned long long epoch = __hip_atomic_load((const GAS unsigned long long*)(ws + WS_EPOCH), __ATOMIC_RELAXED, __HIP_MEMORY_SCOPE_AGENT);
  SB();
  const size_t kvoff = ((size_t)(b * NH + h) * SK + s * KEYS_WG) * 256;
  const rsrc_t rK = mk_rsrc((const char*)kc + kvoff, KEYS_WG * 256);
  const rsrc_t rV = mk_rsrc((const char*)vc + kvoff, KEYS_WG * 256);
  const unsigned loff = w * 1024 + lane * 16;
  v4u st[128];
  auto issue = [&](auto N) {
    constexpr int n = N.v;
    if constexpr (n < 64) st[n] = bld<AUX_NT>(rK, loff, n * 8192);
    else if constexpr (n < 128) st[n] = bld<AUX_NT>(rV, loff, (n - 64) * 8192);
  };
  auto pace = [&](auto N) {
    constexpr int n = N.v;
    asm volatile("" : "+v"(st[n]));
    issue(IC<n + WIN>{});
  };
  static_for<WIN>(issue);
  const int yrow = b * 128 + h * 4 + s;
  if (tid < 8) ((unsigned*)lds)[L_CNT + tid] = 0u;
  SB();
  __syncthreads();
  const Sel selLo = make_sel(c, false);
  unsigned tag32, tlo, thi;
  unsigned long long ep_next;
  auto project = [&](int part) {
    f32x4 pa = {0.f, 0.f, 0.f, 0.f};
#pragma unroll
    for (int i = 0; i < 5; ++i) pa = mfma(WS5[i], sel_frag(XB[i], selLo), pa);
    *(f32x4*)&lds[L_P + w * 256 + lane * 4] = pa;
    LGKM0();
    unsigned old = 0;
    if (lane == 0) old = __hip_atomic_fetch_add((LAS unsigned*)lds + L_CNT + 2 + part, 1u, __ATOMIC_RELAXED, __HIP_MEMORY_SCOPE_WORKGROUP);
    old = __builtin_amdgcn_readfirstlane(old);
    MEMFENCE();
    if (old == 3) {
#pragma unroll
      for (int u = 0; u < 2; ++u) {
        const int idx = (lane + 64 * u) * 2;
        float v0 = 0.f, v1 = 0.f;
#pragma unroll
        for (int k = 0; k < 4; ++k) { const float2 t = *(const float2*)&lds[L_P + (4 * part + k) * 256 + idx]; v0 += t.x; v1 += t.y; }
        const v4u gr = {((__float_as_uint(v0) + 0x80u) & 0xffffff00u) | tlo, thi, ((__float_as_uint(v1) + 0x80u) & 0xffffff00u) | tlo, thi};
        __builtin_amdgcn_raw_buffer_store_b128(gr, rsW, WS_P + (h * 8 + sub) * 4096 + part * 2048 + (lane + 64 * u) * 16, 0, AUX_SC1);
      }
    }
  };
  const unsigned pq_off = WS_P + (h * 8 + w) * 4096 + (g * 16 + b * 8) * 32 + c * 16;
  v4u QG;
  static_for<PF>([&](auto I) {
    constexpr int i = I.v;
    pace(I);
    if constexpr (i == WIN - 1) {
      unsigned elo = (unsigned)epoch, ehi = (unsigned)(epoch >> 32);
      asm volatile("" : "+v"(elo), "+v"(ehi));
      const unsigned long long ep1 = (((unsigned long long)ehi << 32) | elo) + 1ull;
      ep_next = ep1;
      tag32 = (unsigned)ep1; tlo = (unsigned)ep1 & 0xffu; thi = (unsigned)(ep1 >> 8);
      STAMP(8);
      project(w >> 2);
      STAMP(1);
    }
    if constexpr (i == POLL_AT) QG = bld<AUX_SC1>(rsW, pq_off, 0);
    SB();
  });
  auto tagged = [&](const v4u& a) { return ((a.x & 0xffu) ^ tlo) | (a.y ^ thi) | ((a.z & 0xffu) ^ tlo) | (a.w ^ thi); };
  if (!__all(tagged(QG) == 0u)) {
    for (unsigned sp = 0; sp < SPIN_CAP; ++sp) {
      MEMFENCE();
      QG = bld<AUX_SC1>(rsW, pq_off, 0);
      if (__all(tagged(QG) == 0u)) break;
    }
  }
  {
    const int d0 = 32 * g + 16 * (c & 1) + (c >> 1);
    lds[L_Q + w * 128 + d0] = __uint_as_float(QG.x & 0xffffff00u);
    lds[L_Q + w * 128 + d0 + 8] = __uint_as_float(QG.z & 0xffffff00u);
  }
  __syncthreads();
  STAMP(7);
  unsigned q0, q1, q2, q3;
  {
    float qv[8];
    const f32x4* p0 = (const f32x4*)&lds[L_Q + (2 * g) * 128 + 8 * c];
    const f32x4* p1 = (const f32x4*)&lds[L_Q + (2 * g + 1) * 128 + 8 * c];
    const f32x4 a0 = p0[0], a1 = p0[1], b0 = p1[0], b1 = p1[1];
#pragma unroll
    for (int e = 0; e < 4; ++e) { qv[e] = xrow16_sum(a0[e] + b0[e]); qv[4 + e] = xrow16_sum(a1[e] + b1[e]); }
    q0 = cvtpk(qv[0], qv[1]); q1 = cvtpk(qv[2], qv[3]); q2 = cvtpk(qv[4], qv[5]); q3 = cvtpk(qv[6], qv[7]);
  }
  SB();

  const int n0 = has_ffn ? 64 * h + 16 * s : 0;
  const rsrc_t rD = mk_rsrc((const char*)wd + (size_t)n0 * 1280, has_ffn ? 16 * 1280 : 0);
  const rsrc_t rO = mk_rsrc((const char*)wo + (size_t)(h * 128 + 16 * w) * 1280, 16 * 1280);
  const unsigned ovoff = g * 1280 + c * 16;
  const unsigned dvoff = g < 2 ? (2 * w + g) * 1280 + c * 16 : OOB;
  v4u DW[5], OW[20];
  f32x4 ya[5];
  unsigned GX, GW;
  constexpr int OW_K0 = OWK0, OW_STR = OWSTR, OW_NK = (64 - OW_K0 + OW_STR - 1) / OW_STR;

  float sc[4];
  float x1[2], x2[2], x3[2], x4[2];
  auto kcomp = [&](auto I) {
    constexpr int i = I.v;
    if constexpr (i == 26) {
      const int e = 40 * w + (lane < 40 ? lane : 0);
      GX = ((const GAS unsigned*)((const char*)x + b * 1280))[e];
      GW = ((const GAS unsigned*)gw)[e];
      __builtin_amdgcn_raw_buffer_store_b128(v4u{SENT, SENT, SENT, SENT}, rsW, tid < 160 ? WS_Y + yrow * 2560 + tid * 16 : OOB, 0, AUX_SC1);
    }
    if constexpr (i == 36) {
      float gp = bfround(bf2f(GX & 0xffffu) * bf2f(GW & 0xffffu)) + bfround(__uint_as_float(GX & 0xffff0000u) * __uint_as_float(GW & 0xffff0000u));
      gp = wave_sum(lane < 40 ? gp : 0.f);
      lds[L_GP + w] = gp;
    }
    if constexpr (i >= 40 && i < 50 && (i & 1) == 0) DW[(i - 40) / 2] = bld<WNT>(rD, dvoff, ((i - 40) / 2) * 256);
    if constexpr (OW_K0 >= 0 && i >= OW_K0 && (i - OW_K0) % OW_STR == 0) {
      constexpr int f = (i - OW_K0) / OW_STR;
      OW[f] = bld<OWAUX>(rO, ovoff + (f % 5) * 256, (f / 5) * 5120);
    }
    const v4u cur = st[i];
    float d = dot2(cur.x, q0, 0.f);
    d = dot2(cur.y, q1, d); d = dot2(cur.z, q2, d); d = dot2(cur.w, q3, d);
#define MERGE(A, B, CTRL, BIT) merge2<CTRL>(A, B, (c & (BIT)) != 0)
    x1[i & 1] = d;
    if constexpr (i & 1) {
      x2[(i >> 1) & 1] = MERGE(x1[0], x1[1], XOR8, 8);
      if constexpr ((i >> 1) & 1) {
        x3[(i >> 2) & 1] = MERGE(x2[0], x2[1], XOR7, 4);
        if constexpr ((i >> 2) & 1) {
          x4[(i >> 3) & 1] = MERGE(x3[0], x3[1], XOR2, 2);
          if constexpr ((i >> 3) & 1) sc[i / 16] = MERGE(x4[0], x4[1], XOR1, 1);
          if constexpr (!((i >> 3) & 1)) asm volatile("" : "+v"(x4[0]));
        } else asm volatile("" : "+v"(x3[0]));
      } else asm volatile("" : "+v"(x2[0]));
    } else asm volatile("" : "+v"(x1[0]));
#undef MERGE
    if constexpr (i % 16 == 15) asm volatile("" : "+v"(sc[i / 16]));
  };
  static_for<PF>([&](auto T) {
    constexpr int t = T.v;
    pace(IC<PF + t>{});
    kcomp(IC<2 * t>{});
    kcomp(IC<2 * t + 1>{});
    SB();
  });
  static_for<64 - 2 * PF>([&](auto T) {
    constexpr int i = 2 * PF + T.v;
    pace(IC<i>{});
    kcomp(IC<i>{});
    SB();
  });
  STAMP(2);
  constexpr float C2 = 0.08838834764831845f * 1.4426950408889634f;
  const unsigned tag = tag32;
  unsigned PP[4];
  float mC;
  {
    float m = fmaxf(fmaxf(sc[0], sc[1]), fmaxf(sc[2], sc[3]));
    mC = wave_max(m) * C2;
    float l = 0.f;
#pragma unroll
    for (int t = 0; t < 4; ++t) {
      const float p = __builtin_amdgcn_exp2f(fmaf(sc[t], C2, -mC));
      l += p;
      PP[t] = split_hilo(p);
    }
    l = wave_sum(l);
    if (lane == 0) { lds[L_MW + w] = mC; lds[L_LW + w] = l; }
    LGKM0();
    unsigned old = 0;
    if (lane == 0) old = __hip_atomic_fetch_add((LAS unsigned*)lds + L_CNT + 1, 1u, __ATOMIC_RELAXED, __HIP_MEMORY_SCOPE_WORKGROUP);
    old = __builtin_amdgcn_readfirstlane(old);
    MEMFENCE();
    if (old == NWAVE - 1) {
      const f32x4 m0 = *(const f32x4*)&lds[L_MW], m1 = *(const f32x4*)&lds[L_MW + 4];
      const f32x4 l0 = *(const f32x4*)&lds[L_LW], l1 = *(const f32x4*)&lds[L_LW + 4];
      const float M = fmaxf(fmaxf(fmaxf(m0[0], m0[1]), fmaxf(m0[2], m0[3])), fmaxf(fmaxf(m1[0], m1[1]), fmaxf(m1[2], m1[3])));
      float L = 0.f;
#pragma unroll
      for (int k = 0; k < 4; ++k) L += l0[k] * __builtin_amdgcn_exp2f(m0[k] - M) + l1[k] * __builtin_amdgcn_exp2f(m1[k] - M);
      const float lse = M + __builtin_amdgcn_logf(L);
      if (lane == 0)
        __hip_atomic_store((GAS unsigned long long*)(ws + WS_G) + yrow, ((unsigned long long)tag << 32) | __float_as_uint(lse), __ATOMIC_RELAXED, __HIP_MEMORY_SCOPE_AGENT);
    }
  }
  const Sel selM = make_sel(c, c >= 8);

  f32x4 acc = {0.f, 0.f, 0.f, 0.f};
  float gate = 0.f;
  v4u GR = {0, 0, 0, 0}, GR2 = {0, 0, 0, 0};
  float fac = 0.f;
  v4u FG0, FG1;
  unsigned fo0 = 0;
  auto vstep = [&](auto I) {
    constexpr int i = I.v;
    pace(IC<64 + i>{});
    if constexpr (64 + i >= OW_K0 && (64 + i - OW_K0) % OW_STR == 0 && (64 + i - OW_K0) / OW_STR < 20) {
      constexpr int f = (64 + i - OW_K0) / OW_STR;
      OW[f] = bld<OWAUX>(rO, ovoff + (f % 5) * 256, (f / 5) * 5120);
    }
    if constexpr (i == 8) {
      const unsigned pf_off = WS_P + (h * 8 + g) * 4096 + 2048 + ((2 * ((lane >> 3) & 1) + (s >> 1)) * 16 + b * 8 + (lane & 7)) * 32 + (s & 1) * 16;
      fo0 = pf_off; FG0 = bld<AUX_SC1>(rsW, pf_off, 0); FG1 = bld<AUX_SC1>(rsW, pf_off + 4 * 4096, 0);
    }
    if constexpr (i == 40) {
      GR = bld<AUX_SC1>(rsW, WS_G + b * 1024 + lane * 16, 0);
      GR2 = bld<AUX_SC1>(rsW, WS_G + (bid == 0 ? 1024 : b * 1024) + lane * 16, 0);
    }
    constexpr int i4 = i % 16, src = ((i4 & 1) << 3) | ((i4 & 2) << 1) | ((i4 & 4) >> 1) | ((i4 & 8) >> 3);
    const unsigned pb = bcast16<src>(PP[i / 16]);
    acc = mfma(st[64 + i], sel_frag(pb, selM), acc);
    if constexpr (i == 27 || i == 28 || i == 55 || i == 56) asm volatile("" : "+v"(acc));
    if constexpr (i == 28) {
      for (unsigned sp = 0; sp < SPIN_CAP; ++sp) {
        const unsigned cnt = __hip_atomic_load((LAS unsigned*)lds + L_CNT + 1, __ATOMIC_RELAXED, __HIP_MEMORY_SCOPE_WORKGROUP);
        if (__builtin_amdgcn_readfirstlane(cnt) >= NWAVE) break;
        __builtin_amdgcn_s_sleep(1);
      }
      MEMFENCE();
      const f32x4 g0 = *(const f32x4*)&lds[L_GP], g1 = *(const f32x4*)&lds[L_GP + 4];
      const float z = bfround(((g0[0] + g0[1]) + (g0[2] + g0[3])) + ((g1[0] + g1[1]) + (g1[2] + g1[3])));
      gate = bfround(1.f / (1.f + __expf(-z)));
      {
        if (!__all((tagged(FG0) | tagged(FG1)) == 0u)) {
          for (unsigned sp = 0; sp < SPIN_CAP; ++sp) {
            MEMFENCE();
            FG0 = bld<AUX_SC1>(rsW, fo0, 0); FG1 = bld<AUX_SC1>(rsW, fo0 + 4 * 4096, 0);
            if (__all((tagged(FG0) | tagged(FG1)) == 0u)) break;
          }
        }
        const float sx = xrow16_sum(__uint_as_float(FG0.x & 0xffffff00u) + __uint_as_float(FG1.x & 0xffffff00u));
        const float sz = xrow16_sum(__uint_as_float(FG0.z & 0xffffff00u) + __uint_as_float(FG1.z & 0xffffff00u));
        const float sv = w < 4 ? sx : sz;
        const float gt = bfround(sv), up = bfround(dpp<XOR8>(sv));
        const float u = 0.7978845608028654f * (gt + 0.044715f * gt * gt * gt);
        const float th = 1.f - 2.f / (1.f + __expf(2.f * u));
        const float ge = bfround(0.5f * gt * (1.f + th));
        const unsigned hb = cvtpk(ge * up, 0.f);
        const unsigned hv = (unsigned)__builtin_amdgcn_ds_bpermute(((2 * w + (g & 1)) & 7) * 4, (int)hb);
        const v4u bh = sel_frag(hv, selLo);
        const float omg = has_ffn ? bfround(1.f - gate) : 0.f;
#pragma unroll
        for (int k = 0; k < 5; ++k) {
          const f32x4 yf = mfma(DW[k], bh, f32x4{0.f, 0.f, 0.f, 0.f});
#pragma unroll
          for (int r = 0; r < 4; ++r) ya[k][r] = c < 8 ? omg * yf[r] : 0.f;
        }
      }
    }
    if constexpr (i == 56) {
      auto gok = [&]() { return __all(((GR.y ^ tag) | (GR.w ^ tag) | (GR2.y ^ tag) | (GR2.w ^ tag)) == 0u); };
      if (!gok()) {
        for (unsigned sp = 0; sp < SPIN_CAP; ++sp) {
          __builtin_amdgcn_s_sleep(8);
          MEMFENCE();
          GR = bld<AUX_SC1>(rsW, WS_G + b * 1024 + lane * 16, 0);
          GR2 = bld<AUX_SC1>(rsW, WS_G + (bid == 0 ? 1024 : b * 1024) + lane * 16, 0);
          if (gok()) break;
        }
      }
      const float e0 = __uint_as_float(__builtin_amdgcn_readlane(GR.x, 2 * h)), e1 = __uint_as_float(__builtin_amdgcn_readlane(GR.z, 2 * h));
      const float e2 = __uint_as_float(__builtin_amdgcn_readlane(GR.x, 2 * h + 1)), e3 = __uint_as_float(__builtin_amdgcn_readlane(GR.z, 2 * h + 1));
      const float M = fmaxf(fmaxf(e0, e1), fmaxf(e2, e3));
      const float S = (__builtin_amdgcn_exp2f(e0 - M) + __builtin_amdgcn_exp2f(e1 - M)) + (__builtin_amdgcn_exp2f(e2 - M) + __builtin_amdgcn_exp2f(e3 - M));
      fac = gate * __builtin_amdgcn_exp2f(mC - M) / S;
      if (bid == 0 && tid == 0)
        __hip_atomic_store((GAS unsigned long long*)(ws + WS_EPOCH), ep_next, __ATOMIC_RELAXED, __HIP_MEMORY_SCOPE_AGENT);
    }
    SB();
  };
  static_for<64>(vstep);
  STAMP(3);
#pragma unroll
  for (int r = 0; r < 4; ++r) acc[r] = (acc[r] + dpp<XOR8>(acc[r])) * fac;
  if (c < 8) {
#pragma unroll
    for (int r = 0; r < 4; ++r) lds[L_O + (32 * g + 8 * r + c) * 8 + w] = acc[r];
  }
  __syncthreads();
  STAMP(4);
#pragma unroll
  for (int i = 0; i < 4; ++i) {
    const f32x4* op = (const f32x4*)&lds[L_O + (16 * w + 4 * i + g) * 8];
    const f32x4 a = op[0], bb = op[1];
    const float o = ((a[0] + a[1]) + (a[2] + a[3])) + ((bb[0] + bb[1]) + (bb[2] + bb[3]));
    const v4u bf = sel_frag(split_hilo(o), selM);
#pragma unroll
    for (int k = 0; k < 5; ++k) ya[k] = mfma(OW[5 * i + k], bf, ya[k]);
  }
#pragma unroll
  for (int k = 0; k < 5; ++k)
#pragma unroll
    for (int r = 0; r < 4; ++r) {
      const float t = ya[k][r] + dpp<XOR8>(ya[k][r]);
      if (c < 8) lds[L_Y + w * 640 + 128 * k + 32 * g + 8 * r + c] = t;
    }
  __syncthreads();
  if (tid < 160) {
    f32x4 t = *(const f32x4*)&lds[L_Y + 4 * tid];
#pragma unroll
    for (int ww = 1; ww < 8; ++ww) t += *(const f32x4*)&lds[L_Y + ww * 640 + 4 * tid];
    v4u u = __builtin_bit_cast(v4u, t);
#pragma unroll
    for (int e = 0; e < 4; ++e) u[e] = (u[e] == SENT) ? 0x7fc00000u : u[e];
    __builtin_amdgcn_raw_buffer_store_b128(u, rsW, WS_Y + yrow * 2560 + tid * 16, 0, AUX_SC1);
  }
  STAMP(5);
  if (bid < NRED) {
    const int rb = bid & 1, cb = bid >> 1;
    const unsigned off0 = WS_Y + (rb * 128 + 16 * w + (lane >> 3)) * 2560 + cb * 128 + (lane & 7) * 16;
    v4u r0, r1;
    for (unsigned sp = 0; sp < SPIN_CAP; ++sp) {
      MEMFENCE();
      r0 = bld<AUX_SC1>(rsW, off0, 0);
      r1 = bld<AUX_SC1>(rsW, off0 + 8 * 2560, 0);
      const bool ok = (r0.x != SENT) & (r0.y != SENT) & (r0.z != SENT) & (r0.w != SENT) & (r1.x != SENT) & (r1.y != SENT) & (r1.z != SENT) & (r1.w != SENT);
      if (__all(ok)) break;
    }
    f32x4 t = __builtin_bit_cast(f32x4, r0) + __builtin_bit_cast(f32x4, r1);
#pragma unroll
    for (int e = 0; e < 4; ++e) { float v = t[e]; v += dpp<XOR8>(v); t[e] = xrow16_sum(v); }
    if (lane < 8) *(f32x4*)&lds[L_R + w * 32 + lane * 4] = t;
    __syncthreads();
    if (tid < 16) {
      float s0 = 0.f, s1 = 0.f;
#pragma unroll
      for (int ww = 0; ww < 8; ++ww) { s0 += lds[L_R + ww * 32 + 2 * tid]; s1 += lds[L_R + ww * 32 + 2 * tid + 1]; }
      ((unsigned*)out)[rb * 320 + cb * 16 + tid] = cvtpk(s0, s1);
    }
  }
}

extern "C" void kernel_launch(void* const* d_in, const int* in_sizes, int n_in, void* d_out, int out_size, void* d_ws,
                              size_t ws_size, hipStream_t stream) {
  (void)in_sizes; (void)n_in; (void)out_size; (void)ws_size;
  (void)hipFuncSetAttribute((const void*)fused_decode, hipFuncAttributeMaxDynamicSharedMemorySize, LDS_BYTES);
  fused_decode<<<256, NTHR, LDS_BYTES, stream>>>((const u16*)d_in[0], (const u16*)d_in[1], (const u16*)d_in[2], (const u16*)d_in[3],
                                                 (const u16*)d_in[4], (const u16*)d_in[6], (const u16*)d_in[7], (const u16*)d_in[8],
                                                 (const u16*)d_in[9], (u16*)d_out, (unsigned char*)d_ws);
}
```

```cpp
#include <hip/hip_runtime.h>
#include <hip/hip_bf16.h>
#include <stdint.h>
#include <stdio.h>

typedef unsigned short u16;
typedef unsigned v4u __attribute__((ext_vector_type(4)));
typedef unsigned v2u __attribute__((ext_vector_type(2)));
typedef float f32x4 __attribute__((ext_vector_type(4)));
typedef __bf16 bf16x2 __attribute__((ext_vector_type(2)));
typedef __bf16 bf16x8 __attribute__((ext_vector_type(8)));
#define GAS __attribute__((address_space(1)))
#define LAS __attribute__((address_space(3)))
typedef __amdgpu_buffer_rsrc_t rsrc_t;

constexpr int DM = 640, NH = 32, DH = 128, SK = 8192, FF = 1728;
constexpr int NSPLIT = 4, KEYS_WG = SK / NSPLIT, NWAVE = 8, NTHR = NWAVE * 64;
#define WIN 4
#define PF 12
#define POLL_AT (PF - 4)
#define OWK0 8
#define OWSTR 5
#define OWAUX 2
#define KAUX 2
#define VAUX 2
#define WNT 2
constexpr int NGRP_F = FF / 64;
constexpr int AUX_NT = 2, AUX_SC1 = 16;
constexpr int NRED = 40;
constexpr unsigned SENT = 0xFFFFFFFFu;
constexpr unsigned OOB = 0x40000000u;
constexpr unsigned SPIN_CAP = 1u << 18;

constexpr int WS_EPOCH = 0;
constexpr int WS_G = 1024;
constexpr int WS_P = 4096;
constexpr int WS_Y = WS_P + 256 * 4096;
constexpr int WS_BYTES = WS_Y + 256 * 640 * 4;

constexpr int L_P = 0;
constexpr int L_Q = L_P + 8 * 256;
constexpr int L_MW = L_Q + 8 * 128;
constexpr int L_LW = L_MW + 8;
constexpr int L_FP = L_LW + 8;
constexpr int L_GP = L_FP + 8 * 32;
constexpr int L_CNT = L_GP + 8;
constexpr int L_O = L_CNT + 8;
constexpr int L_Y = L_O + 128 * 8;
constexpr int L_R = L_Y + 9 * 640;
constexpr int L_END = L_R + 8 * 32;
constexpr int LDS_BYTES = 96 * 1024;
static_assert(L_END * 4 <= LDS_BYTES, "lds");

__device__ __forceinline__ float dot2(unsigned k, unsigned q, float acc) {
  return __builtin_amdgcn_fdot2_f32_bf16(__builtin_bit_cast(bf16x2, k), __builtin_bit_cast(bf16x2, q), acc, false);
}
__device__ __forceinline__ unsigned cvtpk(float lo, float hi) {
  bf16x2 r = {(__bf16)lo, (__bf16)hi};
  return __builtin_bit_cast(unsigned, r);
}
template <int CTRL> __device__ __forceinline__ float dpp(float x) {
  return __builtin_bit_cast(float, __builtin_amdgcn_mov_dpp(__builtin_bit_cast(int, x), CTRL, 0xf, 0xf, true));
}
template <int CTRL> __device__ __forceinline__ unsigned dppu(unsigned x) {
  return (unsigned)__builtin_amdgcn_mov_dpp((int)x, CTRL, 0xf, 0xf, true);
}
constexpr int XOR1 = 0xB1, XOR2 = 0x4E, XOR7 = 0x141, XOR8 = 0x128, ROR4 = 0x124, NEWBCAST = 0x150;
__device__ __forceinline__ float xrow16_max(float x) {
  auto s = __builtin_amdgcn_permlane16_swap(__float_as_uint(x), __float_as_uint(x), false, false);
  x = fmaxf(__uint_as_float(s[0]), __uint_as_float(s[1]));
  auto t = __builtin_amdgcn_permlane32_swap(__float_as_uint(x), __float_as_uint(x), false, false);
  return fmaxf(__uint_as_float(t[0]), __uint_as_float(t[1]));
}
__device__ __forceinline__ float xrow16_sum(float x) {
  auto s = __builtin_amdgcn_permlane16_swap(__float_as_uint(x), __float_as_uint(x), false, false);
  x = __uint_as_float(s[0]) + __uint_as_float(s[1]);
  auto t = __builtin_amdgcn_permlane32_swap(__float_as_uint(x), __float_as_uint(x), false, false);
  return __uint_as_float(t[0]) + __uint_as_float(t[1]);
}
__device__ __forceinline__ float wave_max(float x) {
  x = fmaxf(x, dpp<XOR1>(x)); x = fmaxf(x, dpp<XOR2>(x)); x = fmaxf(x, dpp<XOR7>(x)); x = fmaxf(x, dpp<XOR8>(x));
  return xrow16_max(x);
}
__device__ __forceinline__ float wave_sum(float x) {
  x += dpp<XOR1>(x); x += dpp<XOR2>(x); x += dpp<XOR7>(x); x += dpp<XOR8>(x);
  return xrow16_sum(x);
}
__device__ __forceinline__ f32x4 mfma(v4u a, v4u b, f32x4 c) {
  return __builtin_amdgcn_mfma_f32_16x16x32_bf16(__builtin_bit_cast(bf16x8, a), __builtin_bit_cast(bf16x8, b), c, 0, 0, 0);
}
__device__ __forceinline__ rsrc_t mk_rsrc(const void* p, unsigned bytes) {
  return __builtin_amdgcn_make_buffer_rsrc((void*)p, 0, bytes, 0x00020000);
}
template <int AUX> __device__ __forceinline__ v4u bld(rsrc_t r, unsigned voff, unsigned soff) {
  return __builtin_amdgcn_raw_buffer_load_b128(r, voff, soff, AUX);
}
__device__ __forceinline__ float bf2f(unsigned lo16) { return __uint_as_float(lo16 << 16); }
__device__ __forceinline__ float bfround(float x) { return (float)(__bf16)x; }
__device__ __forceinline__ unsigned split_hilo(float p) {
  const unsigned t = cvtpk(p, p);
  const float hf = __uint_as_float(t & 0xffff0000u);
  return cvtpk(p, p - hf);
}
template <int CTRL> __device__ __forceinline__ float merge2(float a, float b, bool pick_b) {
  const float ta = a + dpp<CTRL>(a), tb = b + dpp<CTRL>(b);
  return pick_b ? tb : ta;
}
struct Sel { unsigned s[4]; };
__device__ __forceinline__ Sel make_sel(int j, bool pick_hi) {
  Sel r;
  const int e = j & 7;
  const unsigned two = pick_hi ? 0x0302u : 0x0100u;
  const unsigned v = (e & 1) ? ((two << 16) | 0x0c0cu) : (0x0c0c0000u | two);
#pragma unroll
  for (int d = 0; d < 4; ++d) r.s[d] = ((e >> 1) == d) ? v : 0x0c0c0c0cu;
  return r;
}
__device__ __forceinline__ v4u sel_frag(unsigned val, const Sel& s) {
  v4u b;
  b.x = __builtin_amdgcn_perm(val, val, s.s[0]);
  b.y = __builtin_amdgcn_perm(val, val, s.s[1]);
  b.z = __builtin_amdgcn_perm(val, val, s.s[2]);
  b.w = __builtin_amdgcn_perm(val, val, s.s[3]);
  return b;
}
template <int T> __device__ __forceinline__ unsigned bcast16(unsigned x) { return dppu<NEWBCAST + T>(x); }

#define SB() __builtin_amdgcn_sched_barrier(0)
#define MEMFENCE() asm volatile("" ::: "memory")
#define LGKM0() asm volatile("s_waitcnt lgkmcnt(0)" ::: "memory")

template <int I> struct IC { static constexpr int v = I; };
template <int N, int I = 0, class Fn> __device__ __forceinline__ void static_for(Fn&& f) {
  if constexpr (I < N) { f(IC<I>{}); static_for<N, I + 1>(f); }
}
#define STAMP(k) do {} while (0)

__global__ void __launch_bounds__(NTHR, 2)
fused_decode(const u16* __restrict__ x, const u16* __restrict__ kc, const u16* __restrict__ vc, const u16* __restrict__ wq,
             const u16* __restrict__ wo, const u16* __restrict__ wg, const u16* __restrict__ wu, const u16* __restrict__ wd,
             const u16* __restrict__ gw, u16* __restrict__ out, unsigned char* __restrict__ ws) {
  extern __shared__ __attribute__((aligned(16))) float lds[];
  const int tid = threadIdx.x, lane = tid & 63, w = __builtin_amdgcn_readfirstlane(tid >> 6);
  const int bid = blockIdx.x, jj = bid >> 3, h = (bid & 7) + 8 * (jj >> 3), sub = jj & 7, b = sub & 1, s = sub >> 1;
  const int g = lane >> 4, c = lane & 15;
  STAMP(0);
  const rsrc_t rsW = mk_rsrc(ws, WS_BYTES);
  const bool has_ffn = h < NGRP_F;
  const int r0 = 80 * sub + 20 * (w & 3) + g;
  const GAS u16* xp = (const GAS u16*)x + (c >> 3) * DM + r0;
  unsigned XB[5];
  v4u WS5[5];
  {
    const char* wp = (w < 4) ? (const char*)wq + (size_t)r0 * 8192 + h * 256 + c * 16
                             : (const char*)(c < 8 ? wg : wu) + (size_t)r0 * (FF * 2) + (has_ffn ? h : NGRP_F - 1) * 128 + (c & 7) * 16;
    const unsigned rstr = (w < 4) ? 4u * 8192u : 4u * (FF * 2);
#pragma unroll
    for (int i = 0; i < 5; ++i) WS5[i] = __builtin_nontemporal_load((const GAS v4u*)(wp + (size_t)i * rstr));
#pragma unroll
    for (int i = 0; i < 5; ++i) XB[i] = xp[4 * i];
  }
  const unsigned long long epoch = __hip_atomic_load((const GAS unsigned long long*)(ws + WS_EPOCH), __ATOMIC_RELAXED, __HIP_MEMORY_SCOPE_AGENT);
  SB();
  const size_t kvoff = ((size_t)(b * NH + h) * SK + s * KEYS_WG) * 256;
  const rsrc_t rK = mk_rsrc((const char*)kc + kvoff, KEYS_WG * 256);
  const rsrc_t rV = mk_rsrc((const char*)vc + kvoff, KEYS_WG * 256);
  const unsigned loff = w * 1024 + lane * 16;
  v4u st[128];
  auto issue = [&](auto N) {
    constexpr int n = N.v;
    if constexpr (n < 64) st[n] = bld<KAUX>(rK, loff, n * 8192);
    else if constexpr (n < 128) st[n] = bld<VAUX>(rV, loff, (n - 64) * 8192);
  };
  auto pace = [&](auto N) {
    constexpr int n = N.v;
    asm volatile("" : "+v"(st[n]));
    issue(IC<n + WIN>{});
  };
  static_for<WIN>(issue);
  const int yrow = b * 128 + h * 4 + s;
  if (tid < 8) ((unsigned*)lds)[L_CNT + tid] = 0u;
  SB();
  __syncthreads();
  const Sel selLo = make_sel(c, false);
  unsigned tag32, tlo, thi;
  unsigned long long ep_next;
  auto project = [&](int part) {
    f32x4 pa = {0.f, 0.f, 0.f, 0.f};
#pragma unroll
    for (int i = 0; i < 5; ++i) pa = mfma(WS5[i], sel_frag(XB[i], selLo), pa);
    *(f32x4*)&lds[L_P + w * 256 + lane * 4] = pa;
    LGKM0();
    unsigned old = 0;
    if (lane == 0) old = __hip_atomic_fetch_add((LAS unsigned*)lds + L_CNT + 2 + part, 1u, __ATOMIC_RELAXED, __HIP_MEMORY_SCOPE_WORKGROUP);
    old = __builtin_amdgcn_readfirstlane(old);
    MEMFENCE();
    if (old == 3) {
#pragma unroll
      for (int u = 0; u < 2; ++u) {
        const int idx = (lane + 64 * u) * 2;
        float v0 = 0.f, v1 = 0.f;
#pragma unroll
        for (int k = 0; k < 4; ++k) { const float2 t = *(const float2*)&lds[L_P + (4 * part + k) * 256 + idx]; v0 += t.x; v1 += t.y; }
        const v4u gr = {((__float_as_uint(v0) + 0x80u) & 0xffffff00u) | tlo, thi, ((__float_as_uint(v1) + 0x80u) & 0xffffff00u) | tlo, thi};
        __builtin_amdgcn_raw_buffer_store_b128(gr, rsW, WS_P + (h * 8 + sub) * 4096 + part * 2048 + (lane + 64 * u) * 16, 0, AUX_SC1);
      }
    }
  };
  const unsigned pq_off = WS_P + (h * 8 + w) * 4096 + (g * 16 + b * 8) * 32 + c * 16;
  v4u QG;
  static_for<PF>([&](auto I) {
    constexpr int i = I.v;
    pace(I);
    if constexpr (i == WIN - 1) {
      unsigned elo = (unsigned)epoch, ehi = (unsigned)(epoch >> 32);
      asm volatile("" : "+v"(elo), "+v"(ehi));
      const unsigned long long ep1 = (((unsigned long long)ehi << 32) | elo) + 1ull;
      ep_next = ep1;
      tag32 = (unsigned)ep1; tlo = (unsigned)ep1 & 0xffu; thi = (unsigned)(ep1 >> 8);
      STAMP(8);
      project(w >> 2);
      STAMP(1);
    }
    if constexpr (i == POLL_AT) QG = bld<AUX_SC1>(rsW, pq_off, 0);
    SB();
  });
  auto tagged = [&](const v4u& a) { return ((a.x & 0xffu) ^ tlo) | (a.y ^ thi) | ((a.z & 0xffu) ^ tlo) | (a.w ^ thi); };
  if (!__all(tagged(QG) == 0u)) {
    for (unsigned sp = 0; sp < SPIN_CAP; ++sp) {
      MEMFENCE();
      QG = bld<AUX_SC1>(rsW, pq_off, 0);
      if (__all(tagged(QG) == 0u)) break;
    }
  }
  {
    const int d0 = 32 * g + 16 * (c & 1) + (c >> 1);
    lds[L_Q + w * 128 + d0] = __uint_as_float(QG.x & 0xffffff00u);
    lds[L_Q + w * 128 + d0 + 8] = __uint_as_float(QG.z & 0xffffff00u);
  }
  __syncthreads();
  STAMP(7);
  unsigned q0, q1, q2, q3;
  {
    float qv[8];
    const f32x4* p0 = (const f32x4*)&lds[L_Q + (2 * g) * 128 + 8 * c];
    const f32x4* p1 = (const f32x4*)&lds[L_Q + (2 * g + 1) * 128 + 8 * c];
    const f32x4 a0 = p0[0], a1 = p0[1], b0 = p1[0], b1 = p1[1];
#pragma unroll
    for (int e = 0; e < 4; ++e) { qv[e] = xrow16_sum(a0[e] + b0[e]); qv[4 + e] = xrow16_sum(a1[e] + b1[e]); }
    q0 = cvtpk(qv[0], qv[1]); q1 = cvtpk(qv[2], qv[3]); q2 = cvtpk(qv[4], qv[5]); q3 = cvtpk(qv[6], qv[7]);
  }
  SB();

  const int n0 = has_ffn ? 64 * h + 16 * s : 0;
  const rsrc_t rD = mk_rsrc((const char*)wd + (size_t)n0 * 1280, has_ffn ? 16 * 1280 : 0);
  const rsrc_t rO = mk_rsrc((const char*)wo + (size_t)(h * 128 + 16 * w) * 1280, 16 * 1280);
  const unsigned ovoff = g * 1280 + c * 16;
  const unsigned dvoff = g < 2 ? (2 * w + g) * 1280 + c * 16 : OOB;
  v4u DW[5], OW[20];
  f32x4 ya[5];
  unsigned GX, GW;
  constexpr int OW_K0 = OWK0, OW_STR = OWSTR, OW_NK = (64 - OW_K0 + OW_STR - 1) / OW_STR;

  float sc[4];
  float x1[2], x2[2], x3[2], x4[2];
  auto kcomp = [&](auto I) {
    constexpr int i = I.v;
    if constexpr (i == 26) {
      const int e = 40 * w + (lane < 40 ? lane : 0);
      GX = ((const GAS unsigned*)((const char*)x + b * 1280))[e];
      GW = ((const GAS unsigned*)gw)[e];
      __builtin_amdgcn_raw_buffer_store_b128(v4u{SENT, SENT, SENT, SENT}, rsW, tid < 160 ? WS_Y + yrow * 2560 + tid * 16 : OOB, 0, AUX_SC1);
    }
    if constexpr (i == 36) {
      float gp = bfround(bf2f(GX & 0xffffu) * bf2f(GW & 0xffffu)) + bfround(__uint_as_float(GX & 0xffff0000u) * __uint_as_float(GW & 0xffff0000u));
      gp = wave_sum(lane < 40 ? gp : 0.f);
      lds[L_GP + w] = gp;
    }
    if constexpr (i >= 40 && i < 50 && (i & 1) == 0) DW[(i - 40) / 2] = bld<WNT>(rD, dvoff, ((i - 40) / 2) * 256);
    if constexpr (OW_K0 >= 0 && i >= OW_K0 && (i - OW_K0) % OW_STR == 0) {
      constexpr int f = (i - OW_K0) / OW_STR;
      OW[f] = bld<OWAUX>(rO, ovoff + (f % 5) * 256, (f / 5) * 5120);
    }
    const v4u cur = st[i];
    float d = dot2(cur.x, q0, 0.f);
    d = dot2(cur.y, q1, d); d = dot2(cur.z, q2, d); d = dot2(cur.w, q3, d);
#define MERGE(A, B, CTRL, BIT) merge2<CTRL>(A, B, (c & (BIT)) != 0)
    x1[i & 1] = d;
    if constexpr (i & 1) {
      x2[(i >> 1) & 1] = MERGE(x1[0], x1[1], XOR8, 8);
      if constexpr ((i >> 1) & 1) {
        x3[(i >> 2) & 1] = MERGE(x2[0], x2[1], XOR7, 4);
        if constexpr ((i >> 2) & 1) {
          x4[(i >> 3) & 1] = MERGE(x3[0], x3[1], XOR2, 2);
          if constexpr ((i >> 3) & 1) sc[i / 16] = MERGE(x4[0], x4[1], XOR1, 1);
          if constexpr (!((i >> 3) & 1)) asm volatile("" : "+v"(x4[0]));
        } else asm volatile("" : "+v"(x3[0]));
      } else asm volatile("" : "+v"(x2[0]));
    } else asm volatile("" : "+v"(x1[0]));
#undef MERGE
    if constexpr (i % 16 == 15) asm volatile("" : "+v"(sc[i / 16]));
  };
  static_for<PF>([&](auto T) {
    constexpr int t = T.v;
    pace(IC<PF + t>{});
    kcomp(IC<2 * t>{});
    kcomp(IC<2 * t + 1>{});
    SB();
  });
  static_for<64 - 2 * PF>([&](auto T) {
    constexpr int i = 2 * PF + T.v;
    pace(IC<i>{});
    kcomp(IC<i>{});
    SB();
  });
  STAMP(2);
  constexpr float C2 = 0.08838834764831845f * 1.4426950408889634f;
  const unsigned tag = tag32;
  unsigned PP[4];
  float mC;
  {
    float m = fmaxf(fmaxf(sc[0], sc[1]), fmaxf(sc[2], sc[3]));
    mC = wave_max(m) * C2;
    float l = 0.f;
#pragma unroll
    for (int t = 0; t < 4; ++t) {
      const float p = __builtin_amdgcn_exp2f(fmaf(sc[t], C2, -mC));
      l += p;
      PP[t] = split_hilo(p);
    }
    l = wave_sum(l);
    if (lane == 0) { lds[L_MW + w] = mC; lds[L_LW + w] = l; }
    LGKM0();
    unsigned old = 0;
    if (lane == 0) old = __hip_atomic_fetch_add((LAS unsigned*)lds + L_CNT + 1, 1u, __ATOMIC_RELAXED, __HIP_MEMORY_SCOPE_WORKGROUP);
    old = __builtin_amdgcn_readfirstlane(old);
    MEMFENCE();
    if (old == NWAVE - 1) {
      const f32x4 m0 = *(const f32x4*)&lds[L_MW], m1 = *(const f32x4*)&lds[L_MW + 4];
      const f32x4 l0 = *(const f32x4*)&lds[L_LW], l1 = *(const f32x4*)&lds[L_LW + 4];
      const float M = fmaxf(fmaxf(fmaxf(m0[0], m0[1]), fmaxf(m0[2], m0[3])), fmaxf(fmaxf(m1[0], m1[1]), fmaxf(m1[2], m1[3])));
      float L = 0.f;
#pragma unroll
      for (int k = 0; k < 4; ++k) L += l0[k] * __builtin_amdgcn_exp2f(m0[k] - M) + l1[k] * __builtin_amdgcn_exp2f(m1[k] - M);
      const float lse = M + __builtin_amdgcn_logf(L);
      if (lane == 0)
        __hip_atomic_store((GAS unsigned long long*)(ws + WS_G) + yrow, ((unsigned long long)tag << 32) | __float_as_uint(lse), __ATOMIC_RELAXED, __HIP_MEMORY_SCOPE_AGENT);
    }
  }
  const Sel selM = make_sel(c, c >= 8);

  f32x4 acc = {0.f, 0.f, 0.f, 0.f};
  float gate = 0.f;
  v4u GR = {0, 0, 0, 0}, GR2 = {0, 0, 0, 0};
  float fac = 0.f;
  v4u FG0, FG1;
  unsigned fo0 = 0;
  auto vstep = [&](auto I) {
    constexpr int i = I.v;
    pace(IC<64 + i>{});
    if constexpr (64 + i >= OW_K0 && (64 + i - OW_K0) % OW_STR == 0 && (64 + i - OW_K0) / OW_STR < 20) {
      constexpr int f = (64 + i - OW_K0) / OW_STR;
      OW[f] = bld<OWAUX>(rO, ovoff + (f % 5) * 256, (f / 5) * 5120);
    }
    if constexpr (i == 8) {
      const unsigned pf_off = WS_P + (h * 8 + g) * 4096 + 2048 + ((2 * ((lane >> 3) & 1) + (s >> 1)) * 16 + b * 8 + (lane & 7)) * 32 + (s & 1) * 16;
      fo0 = pf_off; FG0 = bld<AUX_SC1>(rsW, pf_off, 0); FG1 = bld<AUX_SC1>(rsW, pf_off + 4 * 4096, 0);
    }
    if constexpr (i == 40) {
      GR = bld<AUX_SC1>(rsW, WS_G + b * 1024 + lane * 16, 0);
      GR2 = bld<AUX_SC1>(rsW, WS_G + (bid == 0 ? 1024 : b * 1024) + lane * 16, 0);
    }
    constexpr int i4 = i % 16, src = ((i4 & 1) << 3) | ((i4 & 2) << 1) | ((i4 & 4) >> 1) | ((i4 & 8) >> 3);
    const unsigned pb = bcast16<src>(PP[i / 16]);
    acc = mfma(st[64 + i], sel_frag(pb, selM), acc);
    if constexpr (i == 27 || i == 28 || i == 55 || i == 56) asm volatile("" : "+v"(acc));
    if constexpr (i == 28) {
      for (unsigned sp = 0; sp < SPIN_CAP; ++sp) {
        const unsigned cnt = __hip_atomic_load((LAS unsigned*)lds + L_CNT + 1, __ATOMIC_RELAXED, __HIP_MEMORY_SCOPE_WORKGROUP);
        if (__builtin_amdgcn_readfirstlane(cnt) >= NWAVE) break;
        __builtin_amdgcn_s_sleep(1);
      }
      MEMFENCE();
      const f32x4 g0 = *(const f32x4*)&lds[L_GP], g1 = *(const f32x4*)&lds[L_GP + 4];
      const float z = bfround(((g0[0] + g0[1]) + (g0[2] + g0[3])) + ((g1[0] + g1[1]) + (g1[2] + g1[3])));
      gate = bfround(1.f / (1.f + __expf(-z)));
      {
        if (!__all((tagged(FG0) | tagged(FG1)) == 0u)) {
          for (unsigned sp = 0; sp < SPIN_CAP; ++sp) {
            MEMFENCE();
            FG0 = bld<AUX_SC1>(rsW, fo0, 0); FG1 = bld<AUX_SC1>(rsW, fo0 + 4 * 4096, 0);
            if (__all((tagged(FG0) | tagged(FG1)) == 0u)) break;
          }
        }
        const float sx = xrow16_sum(__uint_as_float(FG0.x & 0xffffff00u) + __uint_as_float(FG1.x & 0xffffff00u));
        const float sz = xrow16_sum(__uint_as_float(FG0.z & 0xffffff00u) + __uint_as_float(FG1.z & 0xffffff00u));
        const float sv = w < 4 ? sx : sz;
        const float gt = bfround(sv), up = bfround(dpp<XOR8>(sv));
        const float u = 0.7978845608028654f * (gt + 0.044715f * gt * gt * gt);
        const float th = 1.f - 2.f / (1.f + __expf(2.f * u));
        const float ge = bfround(0.5f * gt * (1.f + th));
        const unsigned hb = cvtpk(ge * up, 0.f);
        const unsigned hv = (unsigned)__builtin_amdgcn_ds_bpermute(((2 * w + (g & 1)) & 7) * 4, (int)hb);
        const v4u bh = sel_frag(hv, selLo);
        const float omg = has_ffn ? bfround(1.f - gate) : 0.f;
#pragma unroll
        for (int k = 0; k < 5; ++k) {
          const f32x4 yf = mfma(DW[k], bh, f32x4{0.f, 0.f, 0.f, 0.f});
#pragma unroll
          for (int r = 0; r < 4; ++r) ya[k][r] = c < 8 ? omg * yf[r] : 0.f;
        }
      }
    }
    if constexpr (i == 56) {
      auto gok = [&]() { return __all(((GR.y ^ tag) | (GR.w ^ tag) | (GR2.y ^ tag) | (GR2.w ^ tag)) == 0u); };
      if (!gok()) {
        for (unsigned sp = 0; sp < SPIN_CAP; ++sp) {
          __builtin_amdgcn_s_sleep(8);
          MEMFENCE();
          GR = bld<AUX_SC1>(rsW, WS_G + b * 1024 + lane * 16, 0);
          GR2 = bld<AUX_SC1>(rsW, WS_G + (bid == 0 ? 1024 : b * 1024) + lane * 16, 0);
          if (gok()) break;
        }
      }
      const float e0 = __uint_as_float(__builtin_amdgcn_readlane(GR.x, 2 * h)), e1 = __uint_as_float(__builtin_amdgcn_readlane(GR.z, 2 * h));
      const float e2 = __uint_as_float(__builtin_amdgcn_readlane(GR.x, 2 * h + 1)), e3 = __uint_as_float(__builtin_amdgcn_readlane(GR.z, 2 * h + 1));
      const float M = fmaxf(fmaxf(e0, e1), fmaxf(e2, e3));
      const float S = (__builtin_amdgcn_exp2f(e0 - M) + __builtin_amdgcn_exp2f(e1 - M)) + (__builtin_amdgcn_exp2f(e2 - M) + __builtin_amdgcn_exp2f(e3 - M));
      fac = gate * __builtin_amdgcn_exp2f(mC - M) / S;
      if (bid == 0 && tid == 0)
        __hip_atomic_store((GAS unsigned long long*)(ws + WS_EPOCH), ep_next, __ATOMIC_RELAXED, __HIP_MEMORY_SCOPE_AGENT);
    }
    SB();
  };
  static_for<64>(vstep);
  STAMP(3);
#pragma unroll
  for (int r = 0; r < 4; ++r) acc[r] = (acc[r] + dpp<XOR8>(acc[r])) * fac;
  if (c < 8) {
#pragma unroll
    for (int r = 0; r < 4; ++r) lds[L_O + (32 * g + 8 * r + c) * 8 + w] = acc[r];
  }
  __syncthreads();
  STAMP(4);
#pragma unroll
  for (int i = 0; i < 4; ++i) {
    const f32x4* op = (const f32x4*)&lds[L_O + (16 * w + 4 * i + g) * 8];
    const f32x4 a = op[0], bb = op[1];
    const float o = ((a[0] + a[1]) + (a[2] + a[3])) + ((bb[0] + bb[1]) + (bb[2] + bb[3]));
    const v4u bf = sel_frag(split_hilo(o), selM);
#pragma unroll
    for (int k = 0; k < 5; ++k) ya[k] = mfma(OW[5 * i + k], bf, ya[k]);
  }
#pragma unroll
  for (int k = 0; k < 5; ++k)
#pragma unroll
    for (int r = 0; r < 4; ++r) {
      const float t = ya[k][r] + dpp<XOR8>(ya[k][r]);
      if (c < 8) lds[L_Y + w * 640 + 128 * k + 32 * g + 8 * r + c] = t;
    }
  __syncthreads();
  if (tid < 160) {
    f32x4 t = *(const f32x4*)&lds[L_Y + 4 * tid];
#pragma unroll
    for (int ww = 1; ww < 8; ++ww) t += *(const f32x4*)&lds[L_Y + ww * 640 + 4 * tid];
    v4u u = __builtin_bit_cast(v4u, t);
#pragma unroll
    for (int e = 0; e < 4; ++e) u[e] = (u[e] == SENT) ? 0x7fc00000u : u[e];
    __builtin_amdgcn_raw_buffer_store_b128(u, rsW, WS_Y + yrow * 2560 + tid * 16, 0, AUX_SC1);
  }
  STAMP(5);
  if (bid < NRED) {
    const int rb = bid & 1, cb = bid >> 1;
    const unsigned off0 = WS_Y + (rb * 128 + 16 * w + (lane >> 3)) * 2560 + cb * 128 + (lane & 7) * 16;
    v4u r0, r1;
    for (unsigned sp = 0; sp < SPIN_CAP; ++sp) {
      MEMFENCE();
      r0 = bld<AUX_SC1>(rsW, off0, 0);
      r1 = bld<AUX_SC1>(rsW, off0 + 8 * 2560, 0);
      const bool ok = (r0.x != SENT) & (r0.y != SENT) & (r0.z != SENT) & (r0.w != SENT) & (r1.x != SENT) & (r1.y != SENT) & (r1.z != SENT) & (r1.w != SENT);
      if (__all(ok)) break;
    }
    f32x4 t = __builtin_bit_cast(f32x4, r0) + __builtin_bit_cast(f32x4, r1);
#pragma unroll
    for (int e = 0; e < 4; ++e) { float v = t[e]; v += dpp<XOR8>(v); t[e] = xrow16_sum(v); }
    if (lane < 8) *(f32x4*)&lds[L_R + w * 32 + lane * 4] = t;
    __syncthreads();
    if (tid < 16) {
      float s0 = 0.f, s1 = 0.f;
#pragma unroll
      for (int ww = 0; ww < 8; ++ww) { s0 += lds[L_R + ww * 32 + 2 * tid]; s1 += lds[L_R + ww * 32 + 2 * tid + 1]; }
      ((unsigned*)out)[rb * 320 + cb * 16 + tid] = cvtpk(s0, s1);
    }
  }
}

extern "C" void kernel_launch(void* const* d_in, const int* in_sizes, int n_in, void* d_out, int out_size, void* d_ws,
                              size_t ws_size, hipStream_t stream) {
  (void)in_sizes; (void)n_in; (void)out_size; (void)ws_size;
  for (int i = 0; i < n_in; ++i) fprintf(stderr, "PTR in[%d] = %p size %d\n", i, d_in[i], in_sizes[i]);
  fprintf(stderr, "PTR out = %p ws = %p\n", d_out, d_ws);
  (void)hipFuncSetAttribute((const void*)fused_decode, hipFuncAttributeMaxDynamicSharedMemorySize, LDS_BYTES);
  fused_decode<<<256, NTHR, LDS_BYTES, stream>>>((const u16*)d_in[0], (const u16*)d_in[1], (const u16*)d_in[2], (const u16*)d_in[3],
                                                 (const u16*)d_in[4], (const u16*)d_in[6], (const u16*)d_in[7], (const u16*)d_in[8],
                                                 (const u16*)d_in[9], (u16*)d_out, (unsigned char*)d_ws);
}
```

```cpp
#include <hip/hip_runtime.h>
#include <hip/hip_bf16.h>
#include <stdint.h>

typedef unsigned short u16;
typedef unsigned v4u __attribute__((ext_vector_type(4)));
typedef unsigned v2u __attribute__((ext_vector_type(2)));
typedef float f32x4 __attribute__((ext_vector_type(4)));
typedef __bf16 bf16x2 __attribute__((ext_vector_type(2)));
typedef __bf16 bf16x8 __attribute__((ext_vector_type(8)));
#define GAS __attribute__((address_space(1)))
#define LAS __attribute__((address_space(3)))
typedef __amdgpu_buffer_rsrc_t rsrc_t;

constexpr int DM = 640, NH = 32, DH = 128, SK = 8192, FF = 1728;
constexpr int NSPLIT = 4, KEYS_WG = SK / NSPLIT, NWAVE = 8, NTHR = NWAVE * 64;
#define WIN 4
#define PF 12
#define POLL_AT (PF - 4)
#define OWK0 8
#define OWSTR 5
#define OWAUX 2
#define KAUX 2
#define VAUX 2
#define POLL_SLEEP 4
#define TH_TICKS 28
#define TH_BURST 2
#define TH_EVERY 2
#define FIN_SLEEP 1
#define WNT 2
constexpr int NGRP_F = FF / 64;
constexpr int AUX_NT = 2, AUX_SC1 = 16;
constexpr int NRED = 40;
constexpr unsigned SENT = 0xFFFFFFFFu;
constexpr unsigned OOB = 0x40000000u;
constexpr unsigned SPIN_CAP = 1u << 18;

constexpr int WS_EPOCH = 0;
constexpr int WS_G = 1024;
constexpr int WS_P = 4096;
constexpr int WS_Y = WS_P + 256 * 4096;
constexpr int WS_BYTES = WS_Y + 256 * 640 * 4;

constexpr int L_P = 0;
constexpr int L_Q = L_P + 8 * 256;
constexpr int L_MW = L_Q + 8 * 128;
constexpr int L_LW = L_MW + 8;
constexpr int L_GP = L_LW + 8;
constexpr int L_CNT = L_GP + 8;
constexpr int L_H = L_CNT + 8;
constexpr int L_LSE = L_H + 16;
constexpr int L_O = L_LSE + 4;
constexpr int L_Y = L_O + 128 * 8;
constexpr int L_R = L_Y + 8 * 640;
constexpr int L_END = L_R + 8 * 32;
constexpr int LDS_BYTES = 96 * 1024;
static_assert(L_END * 4 <= LDS_BYTES, "lds");

__device__ __forceinline__ float dot2(unsigned k, unsigned q, float acc) {
  return __builtin_amdgcn_fdot2_f32_bf16(__builtin_bit_cast(bf16x2, k), __builtin_bit_cast(bf16x2, q), acc, false);
}
__device__ __forceinline__ unsigned cvtpk(float lo, float hi) {
  bf16x2 r = {(__bf16)lo, (__bf16)hi};
  return __builtin_bit_cast(unsigned, r);
}
template <int CTRL> __device__ __forceinline__ float dpp(float x) {
  return __builtin_bit_cast(float, __builtin_amdgcn_mov_dpp(__builtin_bit_cast(int, x), CTRL, 0xf, 0xf, true));
}
template <int CTRL> __device__ __forceinline__ unsigned dppu(unsigned x) {
  return (unsigned)__builtin_amdgcn_mov_dpp((int)x, CTRL, 0xf, 0xf, true);
}
constexpr int XOR1 = 0xB1, XOR2 = 0x4E, XOR7 = 0x141, XOR8 = 0x128, ROR4 = 0x124, NEWBCAST = 0x150;
__device__ __forceinline__ float xrow16_max(float x) {
  auto s = __builtin_amdgcn_permlane16_swap(__float_as_uint(x), __float_as_uint(x), false, false);
  x = fmaxf(__uint_as_float(s[0]), __uint_as_float(s[1]));
  auto t = __builtin_amdgcn_permlane32_swap(__float_as_uint(x), __float_as_uint(x), false, false);
  return fmaxf(__uint_as_float(t[0]), __uint_as_float(t[1]));
}
__device__ __forceinline__ float xrow16_sum(float x) {
  auto s = __builtin_amdgcn_permlane16_swap(__float_as_uint(x), __float_as_uint(x), false, false);
  x = __uint_as_float(s[0]) + __uint_as_float(s[1]);
  auto t = __builtin_amdgcn_permlane32_swap(__float_as_uint(x), __float_as_uint(x), false, false);
  return __uint_as_float(t[0]) + __uint_as_float(t[1]);
}
__device__ __forceinline__ float wave_max(float x) {
  x = fmaxf(x, dpp<XOR1>(x)); x = fmaxf(x, dpp<XOR2>(x)); x = fmaxf(x, dpp<XOR7>(x)); x = fmaxf(x, dpp<XOR8>(x));
  return xrow16_max(x);
}
__device__ __forceinline__ float wave_sum(float x) {
  x += dpp<XOR1>(x); x += dpp<XOR2>(x); x += dpp<XOR7>(x); x += dpp<XOR8>(x);
  return xrow16_sum(x);
}
__device__ __forceinline__ f32x4 mfma(v4u a, v4u b, f32x4 c) {
  return __builtin_amdgcn_mfma_f32_16x16x32_bf16(__builtin_bit_cast(bf16x8, a), __builtin_bit_cast(bf16x8, b), c, 0, 0, 0);
}
__device__ __forceinline__ rsrc_t mk_rsrc(const void* p, unsigned bytes) {
  return __builtin_amdgcn_make_buffer_rsrc((void*)p, 0, bytes, 0x00020000);
}
template <int AUX> __device__ __forceinline__ v4u bld(rsrc_t r, unsigned voff, unsigned soff) {
  return __builtin_amdgcn_raw_buffer_load_b128(r, voff, soff, AUX);
}
__device__ __forceinline__ float bf2f(unsigned lo16) { return __uint_as_float(lo16 << 16); }
__device__ __forceinline__ float bfround(float x) { return (float)(__bf16)x; }
__device__ __forceinline__ unsigned split_hilo(float p) {
  const unsigned t = cvtpk(p, p);
  const float hf = __uint_as_float(t & 0xffff0000u);
  return cvtpk(p, p - hf);
}
template <int CTRL> __device__ __forceinline__ float merge2(float a, float b, bool pick_b) {
  const float ta = a + dpp<CTRL>(a), tb = b + dpp<CTRL>(b);
  return pick_b ? tb : ta;
}
struct Sel { unsigned s[4]; };
__device__ __forceinline__ Sel make_sel(int j, bool pick_hi) {
  Sel r;
  const int e = j & 7;
  const unsigned two = pick_hi ? 0x0302u : 0x0100u;
  const unsigned v = (e & 1) ? ((two << 16) | 0x0c0cu) : (0x0c0c0000u | two);
#pragma unroll
  for (int d = 0; d < 4; ++d) r.s[d] = ((e >> 1) == d) ? v : 0x0c0c0c0cu;
  return r;
}
__device__ __forceinline__ v4u sel_frag(unsigned val, const Sel& s) {
  v4u b;
  b.x = __builtin_amdgcn_perm(val, val, s.s[0]);
  b.y = __builtin_amdgcn_perm(val, val, s.s[1]);
  b.z = __builtin_amdgcn_perm(val, val, s.s[2]);
  b.w = __builtin_amdgcn_perm(val, val, s.s[3]);
  return b;
}
template <int T> __device__ __forceinline__ unsigned bcast16(unsigned x) { return dppu<NEWBCAST + T>(x); }

#define SB() __builtin_amdgcn_sched_barrier(0)
#define MEMFENCE() asm volatile("" ::: "memory")
#define LGKM0() asm volatile("s_waitcnt lgkmcnt(0)" ::: "memory")

template <int I> struct IC { static constexpr int v = I; };
template <int N, int I = 0, class Fn> __device__ __forceinline__ void static_for(Fn&& f) {
  if constexpr (I < N) { f(IC<I>{}); static_for<N, I + 1>(f); }
}
#define STAMP(k) do {} while (0)

__global__ void __launch_bounds__(NTHR, 2)
fused_decode(const u16* __restrict__ x, const u16* __restrict__ kc, const u16* __restrict__ vc, const u16* __restrict__ wq,
             const u16* __restrict__ wo, const u16* __restrict__ wg, const u16* __restrict__ wu, const u16* __restrict__ wd,
             const u16* __restrict__ gw, u16* __restrict__ out, unsigned char* __restrict__ ws) {
  extern __shared__ __attribute__((aligned(16))) float lds[];
  const int tid = threadIdx.x, lane = tid & 63, w = __builtin_amdgcn_readfirstlane(tid >> 6);
  const int bid = blockIdx.x, jj = bid >> 3, h = (bid & 7) + 8 * (jj >> 3), sub = jj & 7, b = sub & 1, s = sub >> 1;
  const int g = lane >> 4, c = lane & 15;
  STAMP(0);
  const rsrc_t rsW = mk_rsrc(ws, WS_BYTES);
  const bool has_ffn = h < NGRP_F;
  const int r0 = 80 * sub + 20 * (w & 3) + g;
  const GAS u16* xp = (const GAS u16*)x + (c >> 3) * DM + r0;
  unsigned XB[5];
  v4u WS5[5];
  {
    const char* wp = (w < 4) ? (const char*)wq + (size_t)r0 * 8192 + h * 256 + c * 16
                             : (const char*)(c < 8 ? wg : wu) + (size_t)r0 * (FF * 2) + (has_ffn ? h : NGRP_F - 1) * 128 + (c & 7) * 16;
    const unsigned rstr = (w < 4) ? 4u * 8192u : 4u * (FF * 2);
#pragma unroll
    for (int i = 0; i < 5; ++i) WS5[i] = __builtin_nontemporal_load((const GAS v4u*)(wp + (size_t)i * rstr));
#pragma unroll
    for (int i = 0; i < 5; ++i) XB[i] = xp[4 * i];
  }
  const unsigned long long epoch = __hip_atomic_load((const GAS unsigned long long*)(ws + WS_EPOCH), __ATOMIC_RELAXED, __HIP_MEMORY_SCOPE_AGENT);
  SB();
  const size_t kvoff = ((size_t)(b * NH + h) * SK + s * KEYS_WG) * 256;
  const rsrc_t rK = mk_rsrc((const char*)kc + kvoff, KEYS_WG * 256);
  const rsrc_t rV = mk_rsrc((const char*)vc + kvoff, KEYS_WG * 256);
  const unsigned loff = w * 1024 + lane * 16;
  v4u st[128];
  auto issue = [&](auto N) {
    constexpr int n = N.v;
    if constexpr (n < 64) st[n] = bld<KAUX>(rK, loff, n * 8192);
    else if constexpr (n < 128) st[n] = bld<VAUX>(rV, loff, (n - 64) * 8192);
  };
  unsigned t_q = 0;
  auto pace = [&](auto N) {
    constexpr int n = N.v;
    asm volatile("" : "+v"(st[n]));
    if constexpr (n >= PF + TH_BURST && n + WIN < 128 && (n % TH_EVERY) == 0) {
      while ((unsigned)__builtin_amdgcn_s_memrealtime() - t_q < (unsigned)((n - PF - TH_BURST) * TH_TICKS)) __builtin_amdgcn_s_sleep(1);
    }
    issue(IC<n + WIN>{});
  };
  static_for<WIN>(issue);
  const int yrow = b * 128 + h * 4 + s;
  if (tid < 8) ((unsigned*)lds)[L_CNT + tid] = 0u;
  SB();
  __syncthreads();
  const Sel selLo = make_sel(c, false);
  unsigned tag32, tlo, thi;
  unsigned long long ep_next;
  auto project = [&](int part) {
    f32x4 pa = {0.f, 0.f, 0.f, 0.f};
#pragma unroll
    for (int i = 0; i < 5; ++i) pa = mfma(WS5[i], sel_frag(XB[i], selLo), pa);
#pragma unroll
    for (int r = 0; r < 4; ++r) {
      const int cc = 4 * g + r;
      const int idx = part == 0 ? (c >> 3) * 128 + 8 * cc + (c & 7) : (2 * ((cc & 7) >> 1) + (c >> 3)) * 32 + (cc >> 3) * 16 + 8 * (cc & 1) + (c & 7);
      lds[L_P + w * 256 + idx] = pa[r];
    }
    LGKM0();
    unsigned old = 0;
    if (lane == 0) old = __hip_atomic_fetch_add((LAS unsigned*)lds + L_CNT + 2 + part, 1u, __ATOMIC_RELAXED, __HIP_MEMORY_SCOPE_WORKGROUP);
    old = __builtin_amdgcn_readfirstlane(old);
    MEMFENCE();
    if (old == 3) {
#pragma unroll
      for (int u = 0; u < 2; ++u) {
        const int idx = (lane + 64 * u) * 2;
        float v0 = 0.f, v1 = 0.f;
#pragma unroll
        for (int k = 0; k < 4; ++k) { const float2 t = *(const float2*)&lds[L_P + (4 * part + k) * 256 + idx]; v0 += t.x; v1 += t.y; }
        const v4u gr = {((__float_as_uint(v0) + 0x80u) & 0xffffff00u) | tlo, thi, ((__float_as_uint(v1) + 0x80u) & 0xffffff00u) | tlo, thi};
        __builtin_amdgcn_raw_buffer_store_b128(gr, rsW, WS_P + (h * 8 + sub) * 4096 + part * 2048 + (lane + 64 * u) * 16, 0, AUX_SC1);
      }
    }
  };
  const unsigned pq_off = WS_P + (h * 8 + w) * 4096 + b * 1024 + lane * 16;
  v4u QG;
  static_for<PF>([&](auto I) {
    constexpr int i = I.v;
    pace(I);
    if constexpr (i == WIN - 1) {
      unsigned elo = (unsigned)epoch, ehi = (unsigned)(epoch >> 32);
      asm volatile("" : "+v"(elo), "+v"(ehi));
      const unsigned long long ep1 = (((unsigned long long)ehi << 32) | elo) + 1ull;
      ep_next = ep1;
      tag32 = (unsigned)ep1; tlo = (unsigned)ep1 & 0xffu; thi = (unsigned)(ep1 >> 8);
      STAMP(8);
      project(w >> 2);
      STAMP(1);
    }
    if constexpr (i == POLL_AT) QG = bld<AUX_SC1>(rsW, pq_off, 0);
    SB();
  });
  auto tagged = [&](const v4u& a) { return ((a.x & 0xffu) ^ tlo) | (a.y ^ thi) | ((a.z & 0xffu) ^ tlo) | (a.w ^ thi); };
  if (!__all(tagged(QG) == 0u)) {
    for (unsigned sp = 0; sp < SPIN_CAP; ++sp) {
      __builtin_amdgcn_s_sleep(POLL_SLEEP);
      MEMFENCE();
      QG = bld<AUX_SC1>(rsW, pq_off, 0);
      if (__all(tagged(QG) == 0u)) break;
    }
  }
  *(float2*)&lds[L_Q + w * 128 + 2 * lane] = make_float2(__uint_as_float(QG.x & 0xffffff00u), __uint_as_float(QG.z & 0xffffff00u));
  __syncthreads();
  t_q = (unsigned)__builtin_amdgcn_s_memrealtime();
  STAMP(7);
  unsigned q0, q1, q2, q3;
  {
    float qv[8];
    const f32x4* p0 = (const f32x4*)&lds[L_Q + (2 * g) * 128 + 8 * c];
    const f32x4* p1 = (const f32x4*)&lds[L_Q + (2 * g + 1) * 128 + 8 * c];
    const f32x4 a0 = p0[0], a1 = p0[1], b0 = p1[0], b1 = p1[1];
#pragma unroll
    for (int e = 0; e < 4; ++e) { qv[e] = xrow16_sum(a0[e] + b0[e]); qv[4 + e] = xrow16_sum(a1[e] + b1[e]); }
    q0 = cvtpk(qv[0], qv[1]); q1 = cvtpk(qv[2], qv[3]); q2 = cvtpk(qv[4], qv[5]); q3 = cvtpk(qv[6], qv[7]);
  }
  SB();

  const int n0 = has_ffn ? 64 * h + 16 * s : 0;
  const rsrc_t rD = mk_rsrc((const char*)wd + (size_t)n0 * 1280, has_ffn ? 16 * 1280 : 0);
  const rsrc_t rO = mk_rsrc((const char*)wo + (size_t)(h * 128 + 16 * w) * 1280, 16 * 1280);
  const unsigned ovoff = g * 1280 + c * 16;
  const unsigned dvoff = g < 2 ? (2 * w + g) * 1280 + c * 16 : OOB;
  v4u DW[5], OW[20];
  f32x4 ya[5];
  unsigned GX, GW;
  v4u FG0, FG1;
  const unsigned pf_off = w == 7 ? WS_P + (h * 8 + g) * 4096 + 2048 + sub * 256 + c * 16 : OOB;
  constexpr int OW_K0 = OWK0, OW_STR = OWSTR, OW_NK = (64 - OW_K0 + OW_STR - 1) / OW_STR;

  float sc[4];
  float x1[2], x2[2], x3[2], x4[2];
  auto kcomp = [&](auto I) {
    constexpr int i = I.v;
    if constexpr (i == 26) {
      const int e = 40 * w + (lane < 40 ? lane : 0);
      GX = ((const GAS unsigned*)((const char*)x + b * 1280))[e];
      GW = ((const GAS unsigned*)gw)[e];
      __builtin_amdgcn_raw_buffer_store_b128(v4u{SENT, SENT, SENT, SENT}, rsW, tid < 160 ? WS_Y + yrow * 2560 + tid * 16 : OOB, 0, AUX_SC1);
      FG0 = bld<AUX_SC1>(rsW, pf_off, 0);
      FG1 = bld<AUX_SC1>(rsW, pf_off + (w == 7 ? 4 * 4096 : 0), 0);
    }
    if constexpr (i == 36) {
      float gp = bfround(bf2f(GX & 0xffffu) * bf2f(GW & 0xffffu)) + bfround(__uint_as_float(GX & 0xffff0000u) * __uint_as_float(GW & 0xffff0000u));
      gp = wave_sum(lane < 40 ? gp : 0.f);
      lds[L_GP + w] = gp;
      if (w == 7) {
        if (!__all((tagged(FG0) | tagged(FG1)) == 0u)) {
          for (unsigned sp = 0; sp < SPIN_CAP; ++sp) {
            __builtin_amdgcn_s_sleep(POLL_SLEEP);
            MEMFENCE();
            FG0 = bld<AUX_SC1>(rsW, pf_off, 0); FG1 = bld<AUX_SC1>(rsW, pf_off + 4 * 4096, 0);
            if (__all((tagged(FG0) | tagged(FG1)) == 0u)) break;
          }
        }
        const float s0 = xrow16_sum(__uint_as_float(FG0.x & 0xffffff00u) + __uint_as_float(FG1.x & 0xffffff00u));
        const float s1 = xrow16_sum(__uint_as_float(FG0.z & 0xffffff00u) + __uint_as_float(FG1.z & 0xffffff00u));
        unsigned hb[2];
#pragma unroll
        for (int u2 = 0; u2 < 2; ++u2) {
          const float sv = u2 ? s1 : s0;
          const float gt = bfround(sv), up = bfround(dpp<XOR8>(sv));
          const float u = 0.7978845608028654f * (gt + 0.044715f * gt * gt * gt);
          const float th = 1.f - 2.f / (1.f + __expf(2.f * u));
          const float ge = bfround(0.5f * gt * (1.f + th));
          hb[u2] = cvtpk(ge * up, 0.f);
        }
        if (lane < 8) *(uint2*)&((unsigned*)lds)[L_H + 2 * lane] = make_uint2(hb[0], hb[1]);
      }
    }
    if constexpr (i >= 40 && i < 50 && (i & 1) == 0) DW[(i - 40) / 2] = bld<WNT>(rD, dvoff, ((i - 40) / 2) * 256);
    if constexpr (OW_K0 >= 0 && i >= OW_K0 && (i - OW_K0) % OW_STR == 0) {
      constexpr int f = (i - OW_K0) / OW_STR;
      OW[f] = bld<OWAUX>(rO, ovoff + (f % 5) * 256, (f / 5) * 5120);
    }
    const v4u cur = st[i];
    float d = dot2(cur.x, q0, 0.f);
    d = dot2(cur.y, q1, d); d = dot2(cur.z, q2, d); d = dot2(cur.w, q3, d);
#define MERGE(A, B, CTRL, BIT) merge2<CTRL>(A, B, (c & (BIT)) != 0)
    x1[i & 1] = d;
    if constexpr (i & 1) {
      x2[(i >> 1) & 1] = MERGE(x1[0], x1[1], XOR8, 8);
      if constexpr ((i >> 1) & 1) {
        x3[(i >> 2) & 1] = MERGE(x2[0], x2[1], XOR7, 4);
        if constexpr ((i >> 2) & 1) {
          x4[(i >> 3) & 1] = MERGE(x3[0], x3[1], XOR2, 2);
          if constexpr ((i >> 3) & 1) sc[i / 16] = MERGE(x4[0], x4[1], XOR1, 1);
          if constexpr (!((i >> 3) & 1)) asm volatile("" : "+v"(x4[0]));
        } else asm volatile("" : "+v"(x3[0]));
      } else asm volatile("" : "+v"(x2[0]));
    } else asm volatile("" : "+v"(x1[0]));
#undef MERGE
    if constexpr (i % 16 == 15) asm volatile("" : "+v"(sc[i / 16]));
  };
  static_for<PF>([&](auto T) {
    constexpr int t = T.v;
    pace(IC<PF + t>{});
    kcomp(IC<2 * t>{});
    kcomp(IC<2 * t + 1>{});
    SB();
  });
  static_for<64 - 2 * PF>([&](auto T) {
    constexpr int i = 2 * PF + T.v;
    pace(IC<i>{});
    kcomp(IC<i>{});
    SB();
  });
  STAMP(2);
  constexpr float SCALE = 0.08838834764831845f, LOG2E = 1.4426950408889634f;
  const unsigned tag = tag32;
  unsigned PP[4];
  float mC;
  {
#pragma unroll
    for (int t = 0; t < 4; ++t) sc[t] = bfround(bfround(sc[t]) * SCALE);
    const float m = wave_max(fmaxf(fmaxf(sc[0], sc[1]), fmaxf(sc[2], sc[3])));
    mC = m * LOG2E;
    float l = 0.f;
#pragma unroll
    for (int t = 0; t < 4; ++t) {
      const float p = __builtin_amdgcn_exp2f((sc[t] - m) * LOG2E);
      l += p;
      PP[t] = split_hilo(p);
    }
    l = wave_sum(l);
    if (lane == 0) { lds[L_MW + w] = mC; lds[L_LW + w] = l; }
    LGKM0();
    unsigned old = 0;
    if (lane == 0) old = __hip_atomic_fetch_add((LAS unsigned*)lds + L_CNT + 1, 1u, __ATOMIC_RELAXED, __HIP_MEMORY_SCOPE_WORKGROUP);
    old = __builtin_amdgcn_readfirstlane(old);
    MEMFENCE();
    if (old == NWAVE - 1) {
      const f32x4 m0 = *(const f32x4*)&lds[L_MW], m1 = *(const f32x4*)&lds[L_MW + 4];
      const f32x4 l0 = *(const f32x4*)&lds[L_LW], l1 = *(const f32x4*)&lds[L_LW + 4];
      const float M = fmaxf(fmaxf(fmaxf(m0[0], m0[1]), fmaxf(m0[2], m0[3])), fmaxf(fmaxf(m1[0], m1[1]), fmaxf(m1[2], m1[3])));
      float L = 0.f;
#pragma unroll
      for (int k = 0; k < 4; ++k) L += l0[k] * __builtin_amdgcn_exp2f(m0[k] - M) + l1[k] * __builtin_amdgcn_exp2f(m1[k] - M);
      const float lse = M + __builtin_amdgcn_logf(L);
      if (lane == 0)
        __hip_atomic_store((GAS unsigned long long*)(ws + WS_G) + yrow, ((unsigned long long)tag << 32) | __float_as_uint(lse), __ATOMIC_RELAXED, __HIP_MEMORY_SCOPE_AGENT);
    }
  }
  const Sel selM = make_sel(c, c >= 8);

  f32x4 acc = {0.f, 0.f, 0.f, 0.f};
  float gate = 0.f;
  v4u GR = {0, 0, 0, 0}, GR2 = {0, 0, 0, 0};
  float fac = 0.f;
  const bool gw6 = w == 6, red = bid < NRED;
  const unsigned g_off = gw6 && (b == (bid & 1) && red ? true : (lane >> 1) == h) ? WS_G + b * 1024 + lane * 16 : OOB;
  const unsigned g_off2 = gw6 && red && (b != (bid & 1) || bid == 0) ? WS_G + (bid == 0 ? 1 : (bid & 1)) * 1024 + lane * 16 : OOB;
  auto vstep = [&](auto I) {
    constexpr int i = I.v;
    pace(IC<64 + i>{});
    if constexpr (64 + i >= OW_K0 && (64 + i - OW_K0) % OW_STR == 0 && (64 + i - OW_K0) / OW_STR < 20) {
      constexpr int f = (64 + i - OW_K0) / OW_STR;
      OW[f] = bld<OWAUX>(rO, ovoff + (f % 5) * 256, (f / 5) * 5120);
    }
    if constexpr (i == 36) { GR = bld<AUX_SC1>(rsW, g_off, 0); GR2 = bld<AUX_SC1>(rsW, g_off2, 0); }
    constexpr int i4 = i % 16, src = ((i4 & 1) << 3) | ((i4 & 2) << 1) | ((i4 & 4) >> 1) | ((i4 & 8) >> 3);
    const unsigned pb = bcast16<src>(PP[i / 16]);
    acc = mfma(st[64 + i], sel_frag(pb, selM), acc);
    if constexpr (i == 27 || i == 28 || i == 43 || i == 44 || i == 55 || i == 56) asm volatile("" : "+v"(acc));
    if constexpr (i == 28) {
      for (unsigned sp = 0; sp < SPIN_CAP; ++sp) {
        const unsigned cnt = __hip_atomic_load((LAS unsigned*)lds + L_CNT + 1, __ATOMIC_RELAXED, __HIP_MEMORY_SCOPE_WORKGROUP);
        if (__builtin_amdgcn_readfirstlane(cnt) >= NWAVE) break;
        __builtin_amdgcn_s_sleep(1);
      }
      MEMFENCE();
      const f32x4 g0 = *(const f32x4*)&lds[L_GP], g1 = *(const f32x4*)&lds[L_GP + 4];
      const float z = bfround(((g0[0] + g0[1]) + (g0[2] + g0[3])) + ((g1[0] + g1[1]) + (g1[2] + g1[3])));
      gate = bfround(1.f / (1.f + __expf(-z)));
      {
        const unsigned hv = ((const unsigned*)lds)[L_H + 2 * w + (g & 1)];
        const v4u bh = sel_frag(hv, selLo);
        const float omg = has_ffn ? bfround(1.f - gate) : 0.f;
#pragma unroll
        for (int k = 0; k < 5; ++k) {
          const f32x4 yf = mfma(DW[k], bh, f32x4{0.f, 0.f, 0.f, 0.f});
#pragma unroll
          for (int r = 0; r < 4; ++r) ya[k][r] = c < 8 ? omg * yf[r] : 0.f;
        }
      }
    }
    if constexpr (i == 44) {
      if (gw6) {
        auto bad = [&]() { return (g_off != OOB && ((GR.y ^ tag) | (GR.w ^ tag)) != 0u) || (g_off2 != OOB && ((GR2.y ^ tag) | (GR2.w ^ tag)) != 0u); };
        if (__any(bad())) {
          for (unsigned sp = 0; sp < SPIN_CAP; ++sp) {
            __builtin_amdgcn_s_sleep(8);
            MEMFENCE();
            GR = bld<AUX_SC1>(rsW, g_off, 0); GR2 = bld<AUX_SC1>(rsW, g_off2, 0);
            if (!__any(bad())) break;
          }
        }
        const float e0 = __uint_as_float(__builtin_amdgcn_readlane(GR.x, 2 * h)), e1 = __uint_as_float(__builtin_amdgcn_readlane(GR.z, 2 * h));
        const float e2 = __uint_as_float(__builtin_amdgcn_readlane(GR.x, 2 * h + 1)), e3 = __uint_as_float(__builtin_amdgcn_readlane(GR.z, 2 * h + 1));
        const float M = fmaxf(fmaxf(e0, e1), fmaxf(e2, e3));
        const float S = (__builtin_amdgcn_exp2f(e0 - M) + __builtin_amdgcn_exp2f(e1 - M)) + (__builtin_amdgcn_exp2f(e2 - M) + __builtin_amdgcn_exp2f(e3 - M));
        if (lane == 0) { lds[L_LSE] = M; lds[L_LSE + 1] = S; }
        LGKM0();
        if (lane == 0) __hip_atomic_store((LAS unsigned*)lds + L_CNT + 4, 1u, __ATOMIC_RELAXED, __HIP_MEMORY_SCOPE_WORKGROUP);
        if (bid == 0 && lane == 0)
          __hip_atomic_store((GAS unsigned long long*)(ws + WS_EPOCH), ep_next, __ATOMIC_RELAXED, __HIP_MEMORY_SCOPE_AGENT);
      }
    }
    if constexpr (i == 56) {
      for (unsigned sp = 0; sp < SPIN_CAP; ++sp) {
        const unsigned f = __hip_atomic_load((LAS unsigned*)lds + L_CNT + 4, __ATOMIC_RELAXED, __HIP_MEMORY_SCOPE_WORKGROUP);
        if (__builtin_amdgcn_readfirstlane(f) != 0u) break;
        __builtin_amdgcn_s_sleep(1);
      }
      MEMFENCE();
      fac = gate * __builtin_amdgcn_exp2f(mC - lds[L_LSE]) / lds[L_LSE + 1];
    }
    SB();
  };
  static_for<64>(vstep);
  STAMP(3);
#pragma unroll
  for (int r = 0; r < 4; ++r) acc[r] = (acc[r] + dpp<XOR8>(acc[r])) * fac;
  if (c < 8) {
#pragma unroll
    for (int r = 0; r < 4; ++r) lds[L_O + (32 * g + 8 * r + c) * 8 + w] = acc[r];
  }
  __syncthreads();
  STAMP(4);
#pragma unroll
  for (int i = 0; i < 4; ++i) {
    const f32x4* op = (const f32x4*)&lds[L_O + (16 * w + 4 * i + g) * 8];
    const f32x4 a = op[0], bb = op[1];
    const float o = ((a[0] + a[1]) + (a[2] + a[3])) + ((bb[0] + bb[1]) + (bb[2] + bb[3]));
    const v4u bf = sel_frag(split_hilo(o), selM);
#pragma unroll
    for (int k = 0; k < 5; ++k) ya[k] = mfma(OW[5 * i + k], bf, ya[k]);
  }
#pragma unroll
  for (int k = 0; k < 5; ++k)
#pragma unroll
    for (int r = 0; r < 4; ++r) {
      const float t = ya[k][r] + dpp<XOR8>(ya[k][r]);
      if (c < 8) lds[L_Y + w * 640 + 128 * k + 32 * g + 8 * r + c] = t;
    }
  __syncthreads();
  if (tid < 160) {
    f32x4 t = *(const f32x4*)&lds[L_Y + 4 * tid];
#pragma unroll
    for (int ww = 1; ww < 8; ++ww) t += *(const f32x4*)&lds[L_Y + ww * 640 + 4 * tid];
    v4u u = __builtin_bit_cast(v4u, t);
#pragma unroll
    for (int e = 0; e < 4; ++e) u[e] = (u[e] == SENT) ? 0x7fc00000u : u[e];
    __builtin_amdgcn_raw_buffer_store_b128(u, rsW, WS_Y + yrow * 2560 + tid * 16, 0, AUX_SC1);
  }
  STAMP(5);
  if (bid < NRED) {
    const int rb = bid & 1, cb = bid >> 1;
    const unsigned off0 = WS_Y + (rb * 128 + 16 * w + (lane >> 3)) * 2560 + cb * 128 + (lane & 7) * 16;
    v4u r0 = {SENT, SENT, SENT, SENT}, r1 = {SENT, SENT, SENT, SENT};
    for (unsigned sp = 0; sp < SPIN_CAP; ++sp) {
      MEMFENCE();
      const bool n0_ = (r0.x == SENT) | (r0.y == SENT) | (r0.z == SENT) | (r0.w == SENT), n1_ = (r1.x == SENT) | (r1.y == SENT) | (r1.z == SENT) | (r1.w == SENT);
      const v4u t0 = bld<AUX_SC1>(rsW, n0_ ? off0 : OOB, 0), t1 = bld<AUX_SC1>(rsW, n1_ ? off0 + 8 * 2560 : OOB, 0);
      if (n0_) r0 = t0;
      if (n1_) r1 = t1;
      const bool ok = (r0.x != SENT) & (r0.y != SENT) & (r0.z != SENT) & (r0.w != SENT) & (r1.x != SENT) & (r1.y != SENT) & (r1.z != SENT) & (r1.w != SENT);
      if (__all(ok)) break;
      __builtin_amdgcn_s_sleep(FIN_SLEEP);
    }
    f32x4 t = __builtin_bit_cast(f32x4, r0) + __builtin_bit_cast(f32x4, r1);
#pragma unroll
    for (int e = 0; e < 4; ++e) { float v = t[e]; v += dpp<XOR8>(v); t[e] = xrow16_sum(v); }
    if (lane < 8) *(f32x4*)&lds[L_R + w * 32 + lane * 4] = t;
    __syncthreads();
    if (tid < 16) {
      float s0 = 0.f, s1 = 0.f;
#pragma unroll
      for (int ww = 0; ww < 8; ++ww) { s0 += lds[L_R + ww * 32 + 2 * tid]; s1 += lds[L_R + ww * 32 + 2 * tid + 1]; }
      ((unsigned*)out)[rb * 320 + cb * 16 + tid] = cvtpk(s0, s1);
    }
  }
}

extern "C" void kernel_launch(void* const* d_in, const int* in_sizes, int n_in, void* d_out, int out_size, void* d_ws,
                              size_t ws_size, hipStream_t stream) {
  (void)in_sizes; (void)n_in; (void)out_size; (void)ws_size;
  (void)hipFuncSetAttribute((const void*)fused_decode, hipFuncAttributeMaxDynamicSharedMemorySize, LDS_BYTES);
  fused_decode<<<256, NTHR, LDS_BYTES, stream>>>((const u16*)d_in[0], (const u16*)d_in[1], (const u16*)d_in[2], (const u16*)d_in[3],
                                                 (const u16*)d_in[4], (const u16*)d_in[6], (const u16*)d_in[7], (const u16*)d_in[8],
                                                 (const u16*)d_in[9], (u16*)d_out, (unsigned char*)d_ws);
}
```

```cpp
#include <hip/hip_runtime.h>
#include <hip/hip_bf16.h>
#include <stdint.h>

typedef unsigned short u16;
typedef unsigned v4u __attribute__((ext_vector_type(4)));
typedef unsigned v2u __attribute__((ext_vector_type(2)));
typedef float f32x4 __attribute__((ext_vector_type(4)));
typedef __bf16 bf16x2 __attribute__((ext_vector_type(2)));
typedef __bf16 bf16x8 __attribute__((ext_vector_type(8)));
#define GAS __attribute__((address_space(1)))
#define LAS __attribute__((address_space(3)))
typedef __amdgpu_buffer_rsrc_t rsrc_t;

constexpr int DM = 640, NH = 32, DH = 128, SK = 8192, FF = 1728;
constexpr int NSPLIT = 4, KEYS_WG = SK / NSPLIT, NWAVE = 8, NTHR = NWAVE * 64;
#define WIN 4
#define PF 12
#define POLL_AT (PF - 4)
#define OWK0 8
#define OWSTR 5
#define OWAUX 2
#define KAUX 2
#define VAUX 2
#define POLL_SLEEP 4
#define TH_TICKS 28
#define TH_BURST 2
#define TH_EVERY 2
#define FIN_SLEEP 1
#define WNT 2
constexpr int NGRP_F = FF / 64;
constexpr int AUX_NT = 2, AUX_SC1 = 16;
constexpr int NRED = 40;
constexpr unsigned SENT = 0xFFFFFFFFu;
constexpr unsigned OOB = 0x40000000u;
constexpr unsigned SPIN_CAP = 1u << 18;

constexpr int WS_EPOCH = 0;
constexpr int WS_G = 1024;
constexpr int WS_P = 4096;
constexpr int WS_Y = WS_P + 256 * 4096;
constexpr int WS_BYTES = WS_Y + 256 * 640 * 4;

constexpr int L_P = 0;
constexpr int L_Q = L_P + 8 * 256;
constexpr int L_MW = L_Q + 8 * 128;
constexpr int L_LW = L_MW + 8;
constexpr int L_GP = L_LW + 8;
constexpr int L_CNT = L_GP + 8;
constexpr int L_H = L_CNT + 8;
constexpr int L_LSE = L_H + 16;
constexpr int L_O = L_LSE + 4;
constexpr int L_Y = L_O + 128 * 8;
constexpr int L_R = L_Y + 8 * 640;
constexpr int L_END = L_R + 8 * 32;
constexpr int LDS_BYTES = 96 * 1024;
static_assert(L_END * 4 <= LDS_BYTES, "lds");

__device__ __forceinline__ float dot2(unsigned k, unsigned q, float acc) {
  return __builtin_amdgcn_fdot2_f32_bf16(__builtin_bit_cast(bf16x2, k), __builtin_bit_cast(bf16x2, q), acc, false);
}
__device__ __forceinline__ unsigned cvtpk(float lo, float hi) {
  bf16x2 r = {(__bf16)lo, (__bf16)hi};
  return __builtin_bit_cast(unsigned, r);
}
template <int CTRL> __device__ __forceinline__ float dpp(float x) {
  return __builtin_bit_cast(float, __builtin_amdgcn_mov_dpp(__builtin_bit_cast(int, x), CTRL, 0xf, 0xf, true));
}
template <int CTRL> __device__ __forceinline__ unsigned dppu(unsigned x) {
  return (unsigned)__builtin_amdgcn_mov_dpp((int)x, CTRL, 0xf, 0xf, true);
}
constexpr int XOR1 = 0xB1, XOR2 = 0x4E, XOR7 = 0x141, XOR8 = 0x128, ROR4 = 0x124, NEWBCAST = 0x150;
__device__ __forceinline__ float xrow16_max(float x) {
  auto s = __builtin_amdgcn_permlane16_swap(__float_as_uint(x), __float_as_uint(x), false, false);
  x = fmaxf(__uint_as_float(s[0]), __uint_as_float(s[1]));
  auto t = __builtin_amdgcn_permlane32_swap(__float_as_uint(x), __float_as_uint(x), false, false);
  return fmaxf(__uint_as_float(t[0]), __uint_as_float(t[1]));
}
__device__ __forceinline__ float xrow16_sum(float x) {
  auto s = __builtin_amdgcn_permlane16_swap(__float_as_uint(x), __float_as_uint(x), false, false);
  x = __uint_as_float(s[0]) + __uint_as_float(s[1]);
  auto t = __builtin_amdgcn_permlane32_swap(__float_as_uint(x), __float_as_uint(x), false, false);
  return __uint_as_float(t[0]) + __uint_as_float(t[1]);
}
__device__ __forceinline__ float wave_max(float x) {
  x = fmaxf(x, dpp<XOR1>(x)); x = fmaxf(x, dpp<XOR2>(x)); x = fmaxf(x, dpp<XOR7>(x)); x = fmaxf(x, dpp<XOR8>(x));
  return xrow16_max(x);
}
__device__ __forceinline__ float wave_sum(float x) {
  x += dpp<XOR1>(x); x += dpp<XOR2>(x); x += dpp<XOR7>(x); x += dpp<XOR8>(x);
  return xrow16_sum(x);
}
__device__ __forceinline__ f32x4 mfma(v4u a, v4u b, f32x4 c) {
  return __builtin_amdgcn_mfma_f32_16x16x32_bf16(__builtin_bit_cast(bf16x8, a), __builtin_bit_cast(bf16x8, b), c, 0, 0, 0);
}
__device__ __forceinline__ rsrc_t mk_rsrc(const void* p, unsigned bytes) {
  return __builtin_amdgcn_make_buffer_rsrc((void*)p, 0, bytes, 0x00020000);
}
template <int AUX> __device__ __forceinline__ v4u bld(rsrc_t r, unsigned voff, unsigned soff) {
  return __builtin_amdgcn_raw_buffer_load_b128(r, voff, soff, AUX);
}
__device__ __forceinline__ float bf2f(unsigned lo16) { return __uint_as_float(lo16 << 16); }
__device__ __forceinline__ float bfround(float x) { return (float)(__bf16)x; }
__device__ __forceinline__ unsigned split_hilo(float p) {
  const unsigned t = cvtpk(p, p);
  const float hf = __uint_as_float(t & 0xffff0000u);
  return cvtpk(p, p - hf);
}
template <int CTRL> __device__ __forceinline__ float merge2(float a, float b, bool pick_b) {
  const float ta = a + dpp<CTRL>(a), tb = b + dpp<CTRL>(b);
  return pick_b ? tb : ta;
}
struct Sel { unsigned s[4]; };
__device__ __forceinline__ Sel make_sel(int j, bool pick_hi) {
  Sel r;
  const int e = j & 7;
  const unsigned two = pick_hi ? 0x0302u : 0x0100u;
  const unsigned v = (e & 1) ? ((two << 16) | 0x0c0cu) : (0x0c0c0000u | two);
#pragma unroll
  for (int d = 0; d < 4; ++d) r.s[d] = ((e >> 1) == d) ? v : 0x0c0c0c0cu;
  return r;
}
__device__ __forceinline__ v4u sel_frag(unsigned val, const Sel& s) {
  v4u b;
  b.x = __builtin_amdgcn_perm(val, val, s.s[0]);
  b.y = __builtin_amdgcn_perm(val, val, s.s[1]);
  b.z = __builtin_amdgcn_perm(val, val, s.s[2]);
  b.w = __builtin_amdgcn_perm(val, val, s.s[3]);
  return b;
}
template <int T> __device__ __forceinline__ unsigned bcast16(unsigned x) { return dppu<NEWBCAST + T>(x); }

#define SB() __builtin_amdgcn_sched_barrier(0)
#define MEMFENCE() asm volatile("" ::: "memory")
#define LGKM0() asm volatile("s_waitcnt lgkmcnt(0)" ::: "memory")

template <int I> struct IC { static constexpr int v = I; };
template <int N, int I = 0, class Fn> __device__ __forceinline__ void static_for(Fn&& f) {
  if constexpr (I < N) { f(IC<I>{}); static_for<N, I + 1>(f); }
}
#define STAMP(k) do {} while (0)

__global__ void __launch_bounds__(NTHR, 2)
fused_decode(const u16* __restrict__ x, const u16* __restrict__ kc, const u16* __restrict__ vc, const u16* __restrict__ wq,
             const u16* __restrict__ wo, const u16* __restrict__ wg, const u16* __restrict__ wu, const u16* __restrict__ wd,
             const u16* __restrict__ gw, u16* __restrict__ out, unsigned char* __restrict__ ws) {
  extern __shared__ __attribute__((aligned(16))) float lds[];
  const int tid = threadIdx.x, lane = tid & 63, w = __builtin_amdgcn_readfirstlane(tid >> 6);
  const int bid = blockIdx.x, jj = bid >> 3, h = (bid & 7) + 8 * (jj >> 3), sub = jj & 7, b = sub & 1, s = sub >> 1;
  const int g = lane >> 4, c = lane & 15;
  STAMP(0);
  const unsigned long long dbg_t0 = __builtin_amdgcn_s_memrealtime();
  unsigned long long dbg_t1 = 0;
  const rsrc_t rsW = mk_rsrc(ws, WS_BYTES);
  const bool has_ffn = h < NGRP_F;
  const int r0 = 80 * sub + 20 * (w & 3) + g;
  const GAS u16* xp = (const GAS u16*)x + (c >> 3) * DM + r0;
  unsigned XB[5];
  v4u WS5[5];
  {
    const char* wp = (w < 4) ? (const char*)wq + (size_t)r0 * 8192 + h * 256 + c * 16
                             : (const char*)(c < 8 ? wg : wu) + (size_t)r0 * (FF * 2) + (has_ffn ? h : NGRP_F - 1) * 128 + (c & 7) * 16;
    const unsigned rstr = (w < 4) ? 4u * 8192u : 4u * (FF * 2);
#pragma unroll
    for (int i = 0; i < 5; ++i) WS5[i] = __builtin_nontemporal_load((const GAS v4u*)(wp + (size_t)i * rstr));
#pragma unroll
    for (int i = 0; i < 5; ++i) XB[i] = xp[4 * i];
  }
  const unsigned long long epoch = __hip_atomic_load((const GAS unsigned long long*)(ws + WS_EPOCH), __ATOMIC_RELAXED, __HIP_MEMORY_SCOPE_AGENT);
  SB();
  const size_t kvoff = ((size_t)(b * NH + h) * SK + s * KEYS_WG) * 256;
  const rsrc_t rK = mk_rsrc((const char*)kc + kvoff, KEYS_WG * 256);
  const rsrc_t rV = mk_rsrc((const char*)vc + kvoff, KEYS_WG * 256);
  const unsigned loff = w * 1024 + lane * 16;
  v4u st[128];
  auto issue = [&](auto N) {
    constexpr int n = N.v;
    if constexpr (n < 64) st[n] = bld<KAUX>(rK, loff, n * 8192);
    else if constexpr (n < 128) st[n] = bld<VAUX>(rV, loff, (n - 64) * 8192);
  };
  unsigned t_q = 0;
  auto pace = [&](auto N) {
    constexpr int n = N.v;
    asm volatile("" : "+v"(st[n]));
    if constexpr (n >= PF + TH_BURST && n + WIN < 128 && (n % TH_EVERY) == 0) {
      while ((unsigned)__builtin_amdgcn_s_memrealtime() - t_q < (unsigned)((n - PF - TH_BURST) * TH_TICKS)) __builtin_amdgcn_s_sleep(1);
    }
    issue(IC<n + WIN>{});
  };
  static_for<WIN>(issue);
  const int yrow = b * 128 + h * 4 + s;
  if (tid < 8) ((unsigned*)lds)[L_CNT + tid] = 0u;
  SB();
  __syncthreads();
  const Sel selLo = make_sel(c, false);
  unsigned tag32, tlo, thi;
  unsigned long long ep_next;
  auto project = [&](int part) {
    f32x4 pa = {0.f, 0.f, 0.f, 0.f};
#pragma unroll
    for (int i = 0; i < 5; ++i) pa = mfma(WS5[i], sel_frag(XB[i], selLo), pa);
#pragma unroll
    for (int r = 0; r < 4; ++r) {
      const int cc = 4 * g + r;
      const int idx = part == 0 ? (c >> 3) * 128 + 8 * cc + (c & 7) : (2 * ((cc & 7) >> 1) + (c >> 3)) * 32 + (cc >> 3) * 16 + 8 * (cc & 1) + (c & 7);
      lds[L_P + w * 256 + idx] = pa[r];
    }
    LGKM0();
    unsigned old = 0;
    if (lane == 0) old = __hip_atomic_fetch_add((LAS unsigned*)lds + L_CNT + 2 + part, 1u, __ATOMIC_RELAXED, __HIP_MEMORY_SCOPE_WORKGROUP);
    old = __builtin_amdgcn_readfirstlane(old);
    MEMFENCE();
    if (old == 3) {
#pragma unroll
      for (int u = 0; u < 2; ++u) {
        const int idx = (lane + 64 * u) * 2;
        float v0 = 0.f, v1 = 0.f;
#pragma unroll
        for (int k = 0; k < 4; ++k) { const float2 t = *(const float2*)&lds[L_P + (4 * part + k) * 256 + idx]; v0 += t.x; v1 += t.y; }
        const v4u gr = {((__float_as_uint(v0) + 0x80u) & 0xffffff00u) | tlo, thi, ((__float_as_uint(v1) + 0x80u) & 0xffffff00u) | tlo, thi};
        __builtin_amdgcn_raw_buffer_store_b128(gr, rsW, WS_P + (h * 8 + sub) * 4096 + part * 2048 + (lane + 64 * u) * 16, 0, AUX_SC1);
      }
    }
  };
  const unsigned pq_off = WS_P + (h * 8 + w) * 4096 + b * 1024 + lane * 16;
  v4u QG;
  static_for<PF>([&](auto I) {
    constexpr int i = I.v;
    pace(I);
    if constexpr (i == WIN - 1) {
      unsigned elo = (unsigned)epoch, ehi = (unsigned)(epoch >> 32);
      asm volatile("" : "+v"(elo), "+v"(ehi));
      const unsigned long long ep1 = (((unsigned long long)ehi << 32) | elo) + 1ull;
      ep_next = ep1;
      tag32 = (unsigned)ep1; tlo = (unsigned)ep1 & 0xffu; thi = (unsigned)(ep1 >> 8);
      STAMP(8);
      project(w >> 2);
      STAMP(1);
    }
    if constexpr (i == POLL_AT) QG = bld<AUX_SC1>(rsW, pq_off, 0);
    SB();
  });
  auto tagged = [&](const v4u& a) { return ((a.x & 0xffu) ^ tlo) | (a.y ^ thi) | ((a.z & 0xffu) ^ tlo) | (a.w ^ thi); };
  if (!__all(tagged(QG) == 0u)) {
    for (unsigned sp = 0; sp < SPIN_CAP; ++sp) {
      __builtin_amdgcn_s_sleep(POLL_SLEEP);
      MEMFENCE();
      QG = bld<AUX_SC1>(rsW, pq_off, 0);
      if (__all(tagged(QG) == 0u)) break;
    }
  }
  *(float2*)&lds[L_Q + w * 128 + 2 * lane] = make_float2(__uint_as_float(QG.x & 0xffffff00u), __uint_as_float(QG.z & 0xffffff00u));
  __syncthreads();
  t_q = (unsigned)__builtin_amdgcn_s_memrealtime();
  STAMP(7);
  unsigned q0, q1, q2, q3;
  {
    float qv[8];
    const f32x4* p0 = (const f32x4*)&lds[L_Q + (2 * g) * 128 + 8 * c];
    const f32x4* p1 = (const f32x4*)&lds[L_Q + (2 * g + 1) * 128 + 8 * c];
    const f32x4 a0 = p0[0], a1 = p0[1], b0 = p1[0], b1 = p1[1];
#pragma unroll
    for (int e = 0; e < 4; ++e) { qv[e] = xrow16_sum(a0[e] + b0[e]); qv[4 + e] = xrow16_sum(a1[e] + b1[e]); }
    q0 = cvtpk(qv[0], qv[1]); q1 = cvtpk(qv[2], qv[3]); q2 = cvtpk(qv[4], qv[5]); q3 = cvtpk(qv[6], qv[7]);
  }
  SB();

  const int n0 = has_ffn ? 64 * h + 16 * s : 0;
  const rsrc_t rD = mk_rsrc((const char*)wd + (size_t)n0 * 1280, has_ffn ? 16 * 1280 : 0);
  const rsrc_t rO = mk_rsrc((const char*)wo + (size_t)(h * 128 + 16 * w) * 1280, 16 * 1280);
  const unsigned ovoff = g * 1280 + c * 16;
  const unsigned dvoff = g < 2 ? (2 * w + g) * 1280 + c * 16 : OOB;
  v4u DW[5], OW[20];
  f32x4 ya[5];
  unsigned GX, GW;
  v4u FG0, FG1;
  const unsigned pf_off = w == 7 ? WS_P + (h * 8 + g) * 4096 + 2048 + sub * 256 + c * 16 : OOB;
  constexpr int OW_K0 = OWK0, OW_STR = OWSTR, OW_NK = (64 - OW_K0 + OW_STR - 1) / OW_STR;

  float sc[4];
  float x1[2], x2[2], x3[2], x4[2];
  auto kcomp = [&](auto I) {
    constexpr int i = I.v;
    if constexpr (i == 26) {
      const int e = 40 * w + (lane < 40 ? lane : 0);
      GX = ((const GAS unsigned*)((const char*)x + b * 1280))[e];
      GW = ((const GAS unsigned*)gw)[e];
      __builtin_amdgcn_raw_buffer_store_b128(v4u{SENT, SENT, SENT, SENT}, rsW, tid < 160 ? WS_Y + yrow * 2560 + tid * 16 : OOB, 0, AUX_SC1);
      FG0 = bld<AUX_SC1>(rsW, pf_off, 0);
      FG1 = bld<AUX_SC1>(rsW, pf_off + (w == 7 ? 4 * 4096 : 0), 0);
    }
    if constexpr (i == 36) {
      float gp = bfround(bf2f(GX & 0xffffu) * bf2f(GW & 0xffffu)) + bfround(__uint_as_float(GX & 0xffff0000u) * __uint_as_float(GW & 0xffff0000u));
      gp = wave_sum(lane < 40 ? gp : 0.f);
      lds[L_GP + w] = gp;
      if (w == 7) {
        if (!__all((tagged(FG0) | tagged(FG1)) == 0u)) {
          for (unsigned sp = 0; sp < SPIN_CAP; ++sp) {
            __builtin_amdgcn_s_sleep(POLL_SLEEP);
            MEMFENCE();
            FG0 = bld<AUX_SC1>(rsW, pf_off, 0); FG1 = bld<AUX_SC1>(rsW, pf_off + 4 * 4096, 0);
            if (__all((tagged(FG0) | tagged(FG1)) == 0u)) break;
          }
        }
        const float s0 = xrow16_sum(__uint_as_float(FG0.x & 0xffffff00u) + __uint_as_float(FG1.x & 0xffffff00u));
        const float s1 = xrow16_sum(__uint_as_float(FG0.z & 0xffffff00u) + __uint_as_float(FG1.z & 0xffffff00u));
        unsigned hb[2];
#pragma unroll
        for (int u2 = 0; u2 < 2; ++u2) {
          const float sv = u2 ? s1 : s0;
          const float gt = bfround(sv), up = bfround(dpp<XOR8>(sv));
          const float u = 0.7978845608028654f * (gt + 0.044715f * gt * gt * gt);
          const float th = 1.f - 2.f / (1.f + __expf(2.f * u));
          const float ge = bfround(0.5f * gt * (1.f + th));
          hb[u2] = cvtpk(ge * up, 0.f);
        }
        if (lane < 8) *(uint2*)&((unsigned*)lds)[L_H + 2 * lane] = make_uint2(hb[0], hb[1]);
      }
    }
    if constexpr (i >= 40 && i < 50 && (i & 1) == 0) DW[(i - 40) / 2] = bld<WNT>(rD, dvoff, ((i - 40) / 2) * 256);
    if constexpr (OW_K0 >= 0 && i >= OW_K0 && (i - OW_K0) % OW_STR == 0) {
      constexpr int f = (i - OW_K0) / OW_STR;
      OW[f] = bld<OWAUX>(rO, ovoff + (f % 5) * 256, (f / 5) * 5120);
    }
    const v4u cur = st[i];
    float d = dot2(cur.x, q0, 0.f);
    d = dot2(cur.y, q1, d); d = dot2(cur.z, q2, d); d = dot2(cur.w, q3, d);
#define MERGE(A, B, CTRL, BIT) merge2<CTRL>(A, B, (c & (BIT)) != 0)
    x1[i & 1] = d;
    if constexpr (i & 1) {
      x2[(i >> 1) & 1] = MERGE(x1[0], x1[1], XOR8, 8);
      if constexpr ((i >> 1) & 1) {
        x3[(i >> 2) & 1] = MERGE(x2[0], x2[1], XOR7, 4);
        if constexpr ((i >> 2) & 1) {
          x4[(i >> 3) & 1] = MERGE(x3[0], x3[1], XOR2, 2);
          if constexpr ((i >> 3) & 1) sc[i / 16] = MERGE(x4[0], x4[1], XOR1, 1);
          if constexpr (!((i >> 3) & 1)) asm volatile("" : "+v"(x4[0]));
        } else asm volatile("" : "+v"(x3[0]));
      } else asm volatile("" : "+v"(x2[0]));
    } else asm volatile("" : "+v"(x1[0]));
#undef MERGE
    if constexpr (i % 16 == 15) asm volatile("" : "+v"(sc[i / 16]));
  };
  static_for<PF>([&](auto T) {
    constexpr int t = T.v;
    pace(IC<PF + t>{});
    kcomp(IC<2 * t>{});
    kcomp(IC<2 * t + 1>{});
    SB();
  });
  static_for<64 - 2 * PF>([&](auto T) {
    constexpr int i = 2 * PF + T.v;
    pace(IC<i>{});
    kcomp(IC<i>{});
    SB();
  });
  STAMP(2);
  constexpr float SCALE = 0.08838834764831845f, LOG2E = 1.4426950408889634f;
  const unsigned tag = tag32;
  unsigned PP[4];
  float mC;
  {
#pragma unroll
    for (int t = 0; t < 4; ++t) sc[t] = bfround(bfround(sc[t]) * SCALE);
    const float m = wave_max(fmaxf(fmaxf(sc[0], sc[1]), fmaxf(sc[2], sc[3])));
    mC = m * LOG2E;
    float l = 0.f;
#pragma unroll
    for (int t = 0; t < 4; ++t) {
      const float p = __builtin_amdgcn_exp2f((sc[t] - m) * LOG2E);
      l += p;
      PP[t] = split_hilo(p);
    }
    l = wave_sum(l);
    if (lane == 0) { lds[L_MW + w] = mC; lds[L_LW + w] = l; }
    LGKM0();
    unsigned old = 0;
    if (lane == 0) old = __hip_atomic_fetch_add((LAS unsigned*)lds + L_CNT + 1, 1u, __ATOMIC_RELAXED, __HIP_MEMORY_SCOPE_WORKGROUP);
    old = __builtin_amdgcn_readfirstlane(old);
    MEMFENCE();
    if (old == NWAVE - 1) {
      const f32x4 m0 = *(const f32x4*)&lds[L_MW], m1 = *(const f32x4*)&lds[L_MW + 4];
      const f32x4 l0 = *(const f32x4*)&lds[L_LW], l1 = *(const f32x4*)&lds[L_LW + 4];
      const float M = fmaxf(fmaxf(fmaxf(m0[0], m0[1]), fmaxf(m0[2], m0[3])), fmaxf(fmaxf(m1[0], m1[1]), fmaxf(m1[2], m1[3])));
      float L = 0.f;
#pragma unroll
      for (int k = 0; k < 4; ++k) L += l0[k] * __builtin_amdgcn_exp2f(m0[k] - M) + l1[k] * __builtin_amdgcn_exp2f(m1[k] - M);
      const float lse = M + __builtin_amdgcn_logf(L);
      if (lane == 0)
        __hip_atomic_store((GAS unsigned long long*)(ws + WS_G) + yrow, ((unsigned long long)tag << 32) | __float_as_uint(lse), __ATOMIC_RELAXED, __HIP_MEMORY_SCOPE_AGENT);
    }
  }
  const Sel selM = make_sel(c, c >= 8);

  f32x4 acc = {0.f, 0.f, 0.f, 0.f};
  float gate = 0.f;
  v4u GR = {0, 0, 0, 0}, GR2 = {0, 0, 0, 0};
  float fac = 0.f;
  const bool gw6 = w == 6, red = bid < NRED;
  const unsigned g_off = gw6 && (b == (bid & 1) && red ? true : (lane >> 1) == h) ? WS_G + b * 1024 + lane * 16 : OOB;
  const unsigned g_off2 = gw6 && red && (b != (bid & 1) || bid == 0) ? WS_G + (bid == 0 ? 1 : (bid & 1)) * 1024 + lane * 16 : OOB;
  auto vstep = [&](auto I) {
    constexpr int i = I.v;
    pace(IC<64 + i>{});
    if constexpr (64 + i >= OW_K0 && (64 + i - OW_K0) % OW_STR == 0 && (64 + i - OW_K0) / OW_STR < 20) {
      constexpr int f = (64 + i - OW_K0) / OW_STR;
      OW[f] = bld<OWAUX>(rO, ovoff + (f % 5) * 256, (f / 5) * 5120);
    }
    if constexpr (i == 36) { GR = bld<AUX_SC1>(rsW, g_off, 0); GR2 = bld<AUX_SC1>(rsW, g_off2, 0); }
    constexpr int i4 = i % 16, src = ((i4 & 1) << 3) | ((i4 & 2) << 1) | ((i4 & 4) >> 1) | ((i4 & 8) >> 3);
    const unsigned pb = bcast16<src>(PP[i / 16]);
    acc = mfma(st[64 + i], sel_frag(pb, selM), acc);
    if constexpr (i == 27 || i == 28 || i == 43 || i == 44 || i == 55 || i == 56) asm volatile("" : "+v"(acc));
    if constexpr (i == 28) {
      for (unsigned sp = 0; sp < SPIN_CAP; ++sp) {
        const unsigned cnt = __hip_atomic_load((LAS unsigned*)lds + L_CNT + 1, __ATOMIC_RELAXED, __HIP_MEMORY_SCOPE_WORKGROUP);
        if (__builtin_amdgcn_readfirstlane(cnt) >= NWAVE) break;
        __builtin_amdgcn_s_sleep(1);
      }
      MEMFENCE();
      const f32x4 g0 = *(const f32x4*)&lds[L_GP], g1 = *(const f32x4*)&lds[L_GP + 4];
      const float z = bfround(((g0[0] + g0[1]) + (g0[2] + g0[3])) + ((g1[0] + g1[1]) + (g1[2] + g1[3])));
      gate = bfround(1.f / (1.f + __expf(-z)));
      {
        const unsigned hv = ((const unsigned*)lds)[L_H + 2 * w + (g & 1)];
        const v4u bh = sel_frag(hv, selLo);
        const float omg = has_ffn ? bfround(1.f - gate) : 0.f;
#pragma unroll
        for (int k = 0; k < 5; ++k) {
          const f32x4 yf = mfma(DW[k], bh, f32x4{0.f, 0.f, 0.f, 0.f});
          ya[k] = omg * yf;
          asm volatile("" : "+v"(ya[k]));
        }
      }
    }
    if constexpr (i == 44) {
      if (gw6) {
        auto bad = [&]() { return (g_off != OOB && ((GR.y ^ tag) | (GR.w ^ tag)) != 0u) || (g_off2 != OOB && ((GR2.y ^ tag) | (GR2.w ^ tag)) != 0u); };
        if (__any(bad())) {
          for (unsigned sp = 0; sp < SPIN_CAP; ++sp) {
            __builtin_amdgcn_s_sleep(8);
            MEMFENCE();
            GR = bld<AUX_SC1>(rsW, g_off, 0); GR2 = bld<AUX_SC1>(rsW, g_off2, 0);
            if (!__any(bad())) break;
          }
        }
        const float e0 = __uint_as_float(__builtin_amdgcn_readlane(GR.x, 2 * h)), e1 = __uint_as_float(__builtin_amdgcn_readlane(GR.z, 2 * h));
        const float e2 = __uint_as_float(__builtin_amdgcn_readlane(GR.x, 2 * h + 1)), e3 = __uint_as_float(__builtin_amdgcn_readlane(GR.z, 2 * h + 1));
        const float M = fmaxf(fmaxf(e0, e1), fmaxf(e2, e3));
        const float S = (__builtin_amdgcn_exp2f(e0 - M) + __builtin_amdgcn_exp2f(e1 - M)) + (__builtin_amdgcn_exp2f(e2 - M) + __builtin_amdgcn_exp2f(e3 - M));
        if (lane == 0) { lds[L_LSE] = M; lds[L_LSE + 1] = S; }
        LGKM0();
        if (lane == 0) __hip_atomic_store((LAS unsigned*)lds + L_CNT + 4, 1u, __ATOMIC_RELAXED, __HIP_MEMORY_SCOPE_WORKGROUP);
        if (bid == 0 && lane == 0)
          __hip_atomic_store((GAS unsigned long long*)(ws + WS_EPOCH), ep_next, __ATOMIC_RELAXED, __HIP_MEMORY_SCOPE_AGENT);
      }
    }
    if constexpr (i == 56) {
      for (unsigned sp = 0; sp < SPIN_CAP; ++sp) {
        const unsigned f = __hip_atomic_load((LAS unsigned*)lds + L_CNT + 4, __ATOMIC_RELAXED, __HIP_MEMORY_SCOPE_WORKGROUP);
        if (__builtin_amdgcn_readfirstlane(f) != 0u) break;
        __builtin_amdgcn_s_sleep(1);
      }
      MEMFENCE();
      fac = gate * __builtin_amdgcn_exp2f(mC - lds[L_LSE]) / lds[L_LSE + 1];
    }
    SB();
  };
  static_for<64>(vstep);
  STAMP(3);
  dbg_t1 = __builtin_amdgcn_s_memrealtime();
#pragma unroll
  for (int r = 0; r < 4; ++r) acc[r] = (acc[r] + dpp<XOR8>(acc[r])) * fac;
  if (c < 8) {
#pragma unroll
    for (int r = 0; r < 4; ++r) lds[L_O + (32 * g + 8 * r + c) * 8 + w] = acc[r];
  }
  __syncthreads();
  STAMP(4);
  {
    f32x4 oa[4], ob[4];
#pragma unroll
    for (int i = 0; i < 4; ++i) { const f32x4* op = (const f32x4*)&lds[L_O + (16 * w + 4 * i + g) * 8]; oa[i] = op[0]; ob[i] = op[1]; }
    SB();
#pragma unroll
    for (int i = 0; i < 4; ++i) {
      const float o = ((oa[i][0] + oa[i][1]) + (oa[i][2] + oa[i][3])) + ((ob[i][0] + ob[i][1]) + (ob[i][2] + ob[i][3]));
      const v4u bf = sel_frag(cvtpk(o, o), selLo);
#pragma unroll
      for (int k = 0; k < 5; ++k) ya[k] = mfma(OW[5 * i + k], bf, ya[k]);
    }
  }
  if (c < 8) {
#pragma unroll
    for (int k = 0; k < 5; ++k) *(f32x4*)&lds[L_Y + w * 640 + k * 128 + (g * 8 + c) * 4] = ya[k];
  }
  __syncthreads();
  if (tid < 160) {
    f32x4 t[8];
#pragma unroll
    for (int ww = 0; ww < 8; ++ww) t[ww] = *(const f32x4*)&lds[L_Y + ww * 640 + 4 * tid];
    const f32x4 tt = ((t[0] + t[1]) + (t[2] + t[3])) + ((t[4] + t[5]) + (t[6] + t[7]));
    v4u u = __builtin_bit_cast(v4u, tt);
#pragma unroll
    for (int e = 0; e < 4; ++e) u[e] = (u[e] == SENT) ? 0x7fc00000u : u[e];
    __builtin_amdgcn_raw_buffer_store_b128(u, rsW, WS_Y + yrow * 2560 + tid * 16, 0, AUX_SC1);
  }
  STAMP(5);
  if (bid < NRED) {
    const int rb = bid & 1, cb = bid >> 1;
    const unsigned off0 = WS_Y + (rb * 128 + 16 * w + (lane >> 3)) * 2560 + cb * 128 + (lane & 7) * 16;
    v4u r0 = {SENT, SENT, SENT, SENT}, r1 = {SENT, SENT, SENT, SENT};
    for (unsigned sp = 0; sp < SPIN_CAP; ++sp) {
      MEMFENCE();
      const bool n0_ = (r0.x == SENT) | (r0.y == SENT) | (r0.z == SENT) | (r0.w == SENT), n1_ = (r1.x == SENT) | (r1.y == SENT) | (r1.z == SENT) | (r1.w == SENT);
      const v4u t0 = bld<AUX_SC1>(rsW, n0_ ? off0 : OOB, 0), t1 = bld<AUX_SC1>(rsW, n1_ ? off0 + 8 * 2560 : OOB, 0);
      if (n0_) r0 = t0;
      if (n1_) r1 = t1;
      const bool ok = (r0.x != SENT) & (r0.y != SENT) & (r0.z != SENT) & (r0.w != SENT) & (r1.x != SENT) & (r1.y != SENT) & (r1.z != SENT) & (r1.w != SENT);
      if (__all(ok)) break;
      __builtin_amdgcn_s_sleep(FIN_SLEEP);
    }
    f32x4 t = __builtin_bit_cast(f32x4, r0) + __builtin_bit_cast(f32x4, r1);
#pragma unroll
    for (int e = 0; e < 4; ++e) { float v = t[e]; v += dpp<XOR8>(v); t[e] = xrow16_sum(v); }
    if (lane < 8) *(f32x4*)&lds[L_R + w * 32 + lane * 4] = t;
    __syncthreads();
    if (tid < 16) {
      const int r = tid >> 2, q2 = (tid & 3) * 2;
      float s0 = 0.f, s1 = 0.f;
#pragma unroll
      for (int ww = 0; ww < 8; ++ww) { s0 += lds[L_R + ww * 32 + q2 * 4 + r]; s1 += lds[L_R + ww * 32 + (q2 + 1) * 4 + r]; }
      ((unsigned*)out)[rb * 320 + cb * 16 + 4 * r + (tid & 3)] = cvtpk(s0, s1);
    }
  }
  { const int xq = bid & 7; if (bid == 0) {
    const unsigned long long until = dbg_t0 + 6000 + 2 * ((dbg_t1 - dbg_t0) - 3000);
    while (__builtin_amdgcn_s_memrealtime() < until) __builtin_amdgcn_s_sleep(2);
  } }
}

extern "C" void kernel_launch(void* const* d_in, const int* in_sizes, int n_in, void* d_out, int out_size, void* d_ws,
                              size_t ws_size, hipStream_t stream) {
  (void)in_sizes; (void)n_in; (void)out_size; (void)ws_size;
  (void)hipFuncSetAttribute((const void*)fused_decode, hipFuncAttributeMaxDynamicSharedMemorySize, LDS_BYTES);
  fused_decode<<<256, NTHR, LDS_BYTES, stream>>>((const u16*)d_in[0], (const u16*)d_in[1], (const u16*)d_in[2], (const u16*)d_in[3],
                                                 (const u16*)d_in[4], (const u16*)d_in[6], (const u16*)d_in[7], (const u16*)d_in[8],
                                                 (const u16*)d_in[9], (u16*)d_out, (unsigned char*)d_ws);
}
```

```cpp
#include <hip/hip_runtime.h>
#include <hip/hip_bf16.h>
#include <stdint.h>

typedef unsigned short u16;
typedef unsigned v4u __attribute__((ext_vector_type(4)));
typedef unsigned v2u __attribute__((ext_vector_type(2)));
typedef float f32x4 __attribute__((ext_vector_type(4)));
typedef __bf16 bf16x2 __attribute__((ext_vector_type(2)));
typedef __bf16 bf16x8 __attribute__((ext_vector_type(8)));
#define GAS __attribute__((address_space(1)))
#define LAS __attribute__((address_space(3)))
typedef __amdgpu_buffer_rsrc_t rsrc_t;

constexpr int DM = 640, NH = 32, DH = 128, SK = 8192, FF = 1728;
constexpr int NSPLIT = 4, KEYS_WG = SK / NSPLIT, NWAVE = 8, NTHR = NWAVE * 64;
#define WIN 4
#define PF 12
#define POLL_AT (PF - 4)
#define OWK0 8
#define OWSTR 5
#define OWAUX 2
#define KAUX 2
#define VAUX 2
#define POLL_SLEEP 4
#define TH_TICKS 28
#define TH_BURST 2
#define TH_EVERY 2
#define FIN_SLEEP 1
#define WNT 2
constexpr int NGRP_F = FF / 64;
constexpr int AUX_NT = 2, AUX_SC1 = 16;
constexpr int NRED = 40;
constexpr unsigned SENT = 0xFFFFFFFFu;
constexpr unsigned OOB = 0x40000000u;
constexpr unsigned SPIN_CAP = 1u << 18;

constexpr int WS_EPOCH = 0;
constexpr int WS_G = 1024;
constexpr int WS_P = 4096;
constexpr int WS_Y = WS_P + 256 * 4096;
constexpr int WS_BYTES = WS_Y + 256 * 640 * 4;

constexpr int L_P = 0;
constexpr int L_Q = L_P + 8 * 256;
constexpr int L_MW = L_Q + 8 * 128;
constexpr int L_LW = L_MW + 8;
constexpr int L_GP = L_LW + 8;
constexpr int L_CNT = L_GP + 8;
constexpr int L_H = L_CNT + 8;
constexpr int L_LSE = L_H + 16;
constexpr int L_O = L_LSE + 4;
constexpr int L_Y = L_O + 128 * 8;
constexpr int L_R = L_Y + 8 * 640;
constexpr int L_END = L_R + 8 * 32;
constexpr int LDS_BYTES = 96 * 1024;
static_assert(L_END * 4 <= LDS_BYTES, "lds");

__device__ __forceinline__ float dot2(unsigned k, unsigned q, float acc) {
  return __builtin_amdgcn_fdot2_f32_bf16(__builtin_bit_cast(bf16x2, k), __builtin_bit_cast(bf16x2, q), acc, false);
}
__device__ __forceinline__ unsigned cvtpk(float lo, float hi) {
  bf16x2 r = {(__bf16)lo, (__bf16)hi};
  return __builtin_bit_cast(unsigned, r);
}
template <int CTRL> __device__ __forceinline__ float dpp(float x) {
  return __builtin_bit_cast(float, __builtin_amdgcn_mov_dpp(__builtin_bit_cast(int, x), CTRL, 0xf, 0xf, true));
}
template <int CTRL> __device__ __forceinline__ unsigned dppu(unsigned x) {
  return (unsigned)__builtin_amdgcn_mov_dpp((int)x, CTRL, 0xf, 0xf, true);
}
constexpr int XOR1 = 0xB1, XOR2 = 0x4E, XOR7 = 0x141, XOR8 = 0x128, ROR4 = 0x124, NEWBCAST = 0x150;
__device__ __forceinline__ float xrow16_max(float x) {
  auto s = __builtin_amdgcn_permlane16_swap(__float_as_uint(x), __float_as_uint(x), false, false);
  x = fmaxf(__uint_as_float(s[0]), __uint_as_float(s[1]));
  auto t = __builtin_amdgcn_permlane32_swap(__float_as_uint(x), __float_as_uint(x), false, false);
  return fmaxf(__uint_as_float(t[0]), __uint_as_float(t[1]));
}
__device__ __forceinline__ float xrow16_sum(float x) {
  auto s = __builtin_amdgcn_permlane16_swap(__float_as_uint(x), __float_as_uint(x), false, false);
  x = __uint_as_float(s[0]) + __uint_as_float(s[1]);
  auto t = __builtin_amdgcn_permlane32_swap(__float_as_uint(x), __float_as_uint(x), false, false);
  return __uint_as_float(t[0]) + __uint_as_float(t[1]);
}
__device__ __forceinline__ float wave_max(float x) {
  x = fmaxf(x, dpp<XOR1>(x)); x = fmaxf(x, dpp<XOR2>(x)); x = fmaxf(x, dpp<XOR7>(x)); x = fmaxf(x, dpp<XOR8>(x));
  return xrow16_max(x);
}
__device__ __forceinline__ float wave_sum(float x) {
  x += dpp<XOR1>(x); x += dpp<XOR2>(x); x += dpp<XOR7>(x); x += dpp<XOR8>(x);
  return xrow16_sum(x);
}
__device__ __forceinline__ f32x4 mfma(v4u a, v4u b, f32x4 c) {
  return __builtin_amdgcn_mfma_f32_16x16x32_bf16(__builtin_bit_cast(bf16x8, a), __builtin_bit_cast(bf16x8, b), c, 0, 0, 0);
}
__device__ __forceinline__ rsrc_t mk_rsrc(const void* p, unsigned bytes) {
  return __builtin_amdgcn_make_buffer_rsrc((void*)p, 0, bytes, 0x00020000);
}
template <int AUX> __device__ __forceinline__ v4u bld(rsrc_t r, unsigned voff, unsigned soff) {
  return __builtin_amdgcn_raw_buffer_load_b128(r, voff, soff, AUX);
}
__device__ __forceinline__ float bf2f(unsigned lo16) { return __uint_as_float(lo16 << 16); }
__device__ __forceinline__ float bfround(float x) { return (float)(__bf16)x; }
__device__ __forceinline__ unsigned split_hilo(float p) {
  const unsigned t = cvtpk(p, p);
  const float hf = __uint_as_float(t & 0xffff0000u);
  return cvtpk(p, p - hf);
}
template <int CTRL> __device__ __forceinline__ float merge2(float a, float b, bool pick_b) {
  const float ta = a + dpp<CTRL>(a), tb = b + dpp<CTRL>(b);
  return pick_b ? tb : ta;
}
struct Sel { unsigned s[4]; };
__device__ __forceinline__ Sel make_sel(int j, bool pick_hi) {
  Sel r;
  const int e = j & 7;
  const unsigned two = pick_hi ? 0x0302u : 0x0100u;
  const unsigned v = (e & 1) ? ((two << 16) | 0x0c0cu) : (0x0c0c0000u | two);
#pragma unroll
  for (int d = 0; d < 4; ++d) r.s[d] = ((e >> 1) == d) ? v : 0x0c0c0c0cu;
  return r;
}
__device__ __forceinline__ v4u sel_frag(unsigned val, const Sel& s) {
  v4u b;
  b.x = __builtin_amdgcn_perm(val, val, s.s[0]);
  b.y = __builtin_amdgcn_perm(val, val, s.s[1]);
  b.z = __builtin_amdgcn_perm(val, val, s.s[2]);
  b.w = __builtin_amdgcn_perm(val, val, s.s[3]);
  return b;
}
template <int T> __device__ __forceinline__ unsigned bcast16(unsigned x) { return dppu<NEWBCAST + T>(x); }

#define SB() __builtin_amdgcn_sched_barrier(0)
#define MEMFENCE() asm volatile("" ::: "memory")
#define LGKM0() asm volatile("s_waitcnt lgkmcnt(0)" ::: "memory")

template <int I> struct IC { static constexpr int v = I; };
template <int N, int I = 0, class Fn> __device__ __forceinline__ void static_for(Fn&& f) {
  if constexpr (I < N) { f(IC<I>{}); static_for<N, I + 1>(f); }
}
#define STAMP(k) do {} while (0)

__global__ void __launch_bounds__(NTHR, 2)
fused_decode(const u16* __restrict__ x, const u16* __restrict__ kc, const u16* __restrict__ vc, const u16* __restrict__ wq,
             const u16* __restrict__ wo, const u16* __restrict__ wg, const u16* __restrict__ wu, const u16* __restrict__ wd,
             const u16* __restrict__ gw, u16* __restrict__ out, unsigned char* __restrict__ ws) {
  extern __shared__ __attribute__((aligned(16))) float lds[];
  const int tid = threadIdx.x, lane = tid & 63, w = __builtin_amdgcn_readfirstlane(tid >> 6);
  const int bid = blockIdx.x, jj = bid >> 3, h = (bid & 7) + 8 * (jj >> 3), sub = jj & 7, b = sub & 1, s = sub >> 1;
  const int g = lane >> 4, c = lane & 15;
  STAMP(0);
  const rsrc_t rsW = mk_rsrc(ws, WS_BYTES);
  const bool has_ffn = h < NGRP_F;
  const int r0 = 80 * sub + 20 * (w & 3) + g;
  const GAS u16* xp = (const GAS u16*)x + (c >> 3) * DM + r0;
  unsigned XB[5];
  v4u WS5[5];
  {
    const char* wp = (w < 4) ? (const char*)wq + (size_t)r0 * 8192 + h * 256 + c * 16
                             : (const char*)(c < 8 ? wg : wu) + (size_t)r0 * (FF * 2) + (has_ffn ? h : NGRP_F - 1) * 128 + (c & 7) * 16;
    const unsigned rstr = (w < 4) ? 4u * 8192u : 4u * (FF * 2);
#pragma unroll
    for (int i = 0; i < 5; ++i) WS5[i] = __builtin_nontemporal_load((const GAS v4u*)(wp + (size_t)i * rstr));
#pragma unroll
    for (int i = 0; i < 5; ++i) XB[i] = xp[4 * i];
  }
  const unsigned long long epoch = __hip_atomic_load((const GAS unsigned long long*)(ws + WS_EPOCH), __ATOMIC_RELAXED, __HIP_MEMORY_SCOPE_AGENT);
  SB();
  const size_t kvoff = ((size_t)(b * NH + h) * SK + s * KEYS_WG) * 256;
  const rsrc_t rK = mk_rsrc((const char*)kc + kvoff, KEYS_WG * 256);
  const rsrc_t rV = mk_rsrc((const char*)vc + kvoff, KEYS_WG * 256);
  const unsigned loff = w * 1024 + lane * 16;
  v4u st[128];
  auto issue = [&](auto N) {
    constexpr int n = N.v;
    if constexpr (n < 64) st[n] = bld<KAUX>(rK, loff, n * 8192);
    else if constexpr (n < 128) st[n] = bld<VAUX>(rV, loff, (n - 64) * 8192);
  };
  unsigned t_q = 0;
  auto pace = [&](auto N) {
    constexpr int n = N.v;
    asm volatile("" : "+v"(st[n]));
    if constexpr (n >= PF + TH_BURST && n + WIN < 128 && (n % TH_EVERY) == 0) {
      while ((unsigned)__builtin_amdgcn_s_memrealtime() - t_q < (unsigned)((n - PF - TH_BURST) * TH_TICKS)) __builtin_amdgcn_s_sleep(1);
    }
    issue(IC<n + WIN>{});
  };
  static_for<WIN>(issue);
  const int yrow = b * 128 + h * 4 + s;
  if (tid < 8) ((unsigned*)lds)[L_CNT + tid] = 0u;
  SB();
  __syncthreads();
  const Sel selLo = make_sel(c, false);
  unsigned tag32, tlo, thi;
  unsigned long long ep_next;
  auto project = [&](int part) {
    f32x4 pa = {0.f, 0.f, 0.f, 0.f};
#pragma unroll
    for (int i = 0; i < 5; ++i) pa = mfma(WS5[i], sel_frag(XB[i], selLo), pa);
#pragma unroll
    for (int r = 0; r < 4; ++r) {
      const int cc = 4 * g + r;
      const int idx = part == 0 ? (c >> 3) * 128 + 8 * cc + (c & 7) : (2 * ((cc & 7) >> 1) + (c >> 3)) * 32 + (cc >> 3) * 16 + 8 * (cc & 1) + (c & 7);
      lds[L_P + w * 256 + idx] = pa[r];
    }
    LGKM0();
    unsigned old = 0;
    if (lane == 0) old = __hip_atomic_fetch_add((LAS unsigned*)lds + L_CNT + 2 + part, 1u, __ATOMIC_RELAXED, __HIP_MEMORY_SCOPE_WORKGROUP);
    old = __builtin_amdgcn_readfirstlane(old);
    MEMFENCE();
    if (old == 3) {
#pragma unroll
      for (int u = 0; u < 2; ++u) {
        const int idx = (lane + 64 * u) * 2;
        float v0 = 0.f, v1 = 0.f;
#pragma unroll
        for (int k = 0; k < 4; ++k) { const float2 t = *(const float2*)&lds[L_P + (4 * part + k) * 256 + idx]; v0 += t.x; v1 += t.y; }
        const v4u gr = {((__float_as_uint(v0) + 0x80u) & 0xffffff00u) | tlo, thi, ((__float_as_uint(v1) + 0x80u) & 0xffffff00u) | tlo, thi};
        __builtin_amdgcn_raw_buffer_store_b128(gr, rsW, WS_P + (h * 8 + sub) * 4096 + part * 2048 + (lane + 64 * u) * 16, 0, AUX_SC1);
      }
    }
  };
  const unsigned pq_off = WS_P + (h * 8 + w) * 4096 + b * 1024 + lane * 16;
  v4u QG;
  static_for<PF>([&](auto I) {
    constexpr int i = I.v;
    pace(I);
    if constexpr (i == WIN - 1) {
      unsigned elo = (unsigned)epoch, ehi = (unsigned)(epoch >> 32);
      asm volatile("" : "+v"(elo), "+v"(ehi));
      const unsigned long long ep1 = (((unsigned long long)ehi << 32) | elo) + 1ull;
      ep_next = ep1;
      tag32 = (unsigned)ep1; tlo = (unsigned)ep1 & 0xffu; thi = (unsigned)(ep1 >> 8);
      STAMP(8);
      project(w >> 2);
      STAMP(1);
    }
    if constexpr (i == POLL_AT) QG = bld<AUX_SC1>(rsW, pq_off, 0);
    SB();
  });
  auto tagged = [&](const v4u& a) { return ((a.x & 0xffu) ^ tlo) | (a.y ^ thi) | ((a.z & 0xffu) ^ tlo) | (a.w ^ thi); };
  if (!__all(tagged(QG) == 0u)) {
    for (unsigned sp = 0; sp < SPIN_CAP; ++sp) {
      __builtin_amdgcn_s_sleep(POLL_SLEEP);
      MEMFENCE();
      QG = bld<AUX_SC1>(rsW, pq_off, 0);
      if (__all(tagged(QG) == 0u)) break;
    }
  }
  *(float2*)&lds[L_Q + w * 128 + 2 * lane] = make_float2(__uint_as_float(QG.x & 0xffffff00u), __uint_as_float(QG.z & 0xffffff00u));
  __syncthreads();
  t_q = (unsigned)__builtin_amdgcn_s_memrealtime();
  STAMP(7);
  unsigned q0, q1, q2, q3;
  {
    float qv[8];
    const f32x4* p0 = (const f32x4*)&lds[L_Q + (2 * g) * 128 + 8 * c];
    const f32x4* p1 = (const f32x4*)&lds[L_Q + (2 * g + 1) * 128 + 8 * c];
    const f32x4 a0 = p0[0], a1 = p0[1], b0 = p1[0], b1 = p1[1];
#pragma unroll
    for (int e = 0; e < 4; ++e) { qv[e] = xrow16_sum(a0[e] + b0[e]); qv[4 + e] = xrow16_sum(a1[e] + b1[e]); }
    q0 = cvtpk(qv[0], qv[1]); q1 = cvtpk(qv[2], qv[3]); q2 = cvtpk(qv[4], qv[5]); q3 = cvtpk(qv[6], qv[7]);
  }
  SB();

  const int n0 = has_ffn ? 64 * h + 16 * s : 0;
  const rsrc_t rD = mk_rsrc((const char*)wd + (size_t)n0 * 1280, has_ffn ? 16 * 1280 : 0);
  const rsrc_t rO = mk_rsrc((const char*)wo + (size_t)(h * 128 + 16 * w) * 1280, 16 * 1280);
  const unsigned ovoff = g * 1280 + c * 16;
  const unsigned dvoff = g < 2 ? (2 * w + g) * 1280 + c * 16 : OOB;
  v4u DW[5], OW[20];
  f32x4 ya[5];
  unsigned GX, GW;
  v4u FG0, FG1;
  const unsigned pf_off = w == 7 ? WS_P + (h * 8 + g) * 4096 + 2048 + sub * 256 + c * 16 : OOB;
  constexpr int OW_K0 = OWK0, OW_STR = OWSTR, OW_NK = (64 - OW_K0 + OW_STR - 1) / OW_STR;

  float sc[4];
  float x1[2], x2[2], x3[2], x4[2];
  auto kcomp = [&](auto I) {
    constexpr int i = I.v;
    if constexpr (i == 26) {
      const int e = 40 * w + (lane < 40 ? lane : 0);
      GX = ((const GAS unsigned*)((const char*)x + b * 1280))[e];
      GW = ((const GAS unsigned*)gw)[e];
      __builtin_amdgcn_raw_buffer_store_b128(v4u{SENT, SENT, SENT, SENT}, rsW, tid < 160 ? WS_Y + yrow * 2560 + tid * 16 : OOB, 0, AUX_SC1);
      FG0 = bld<AUX_SC1>(rsW, pf_off, 0);
      FG1 = bld<AUX_SC1>(rsW, pf_off + (w == 7 ? 4 * 4096 : 0), 0);
    }
    if constexpr (i == 36) {
      float gp = bfround(bf2f(GX & 0xffffu) * bf2f(GW & 0xffffu)) + bfround(__uint_as_float(GX & 0xffff0000u) * __uint_as_float(GW & 0xffff0000u));
      gp = wave_sum(lane < 40 ? gp : 0.f);
      lds[L_GP + w] = gp;
      if (w == 7) {
        if (!__all((tagged(FG0) | tagged(FG1)) == 0u)) {
          for (unsigned sp = 0; sp < SPIN_CAP; ++sp) {
            __builtin_amdgcn_s_sleep(POLL_SLEEP);
            MEMFENCE();
            FG0 = bld<AUX_SC1>(rsW, pf_off, 0); FG1 = bld<AUX_SC1>(rsW, pf_off + 4 * 4096, 0);
            if (__all((tagged(FG0) | tagged(FG1)) == 0u)) break;
          }
        }
        const float s0 = xrow16_sum(__uint_as_float(FG0.x & 0xffffff00u) + __uint_as_float(FG1.x & 0xffffff00u));
        const float s1 = xrow16_sum(__uint_as_float(FG0.z & 0xffffff00u) + __uint_as_float(FG1.z & 0xffffff00u));
        unsigned hb[2];
#pragma unroll
        for (int u2 = 0; u2 < 2; ++u2) {
          const float sv = u2 ? s1 : s0;
          const float gt = bfround(sv), up = bfround(dpp<XOR8>(sv));
          const float u = 0.7978845608028654f * (gt + 0.044715f * gt * gt * gt);
          const float th = 1.f - 2.f / (1.f + __expf(2.f * u));
          const float ge = bfround(0.5f * gt * (1.f + th));
          hb[u2] = cvtpk(ge * up, 0.f);
        }
        if (lane < 8) *(uint2*)&((unsigned*)lds)[L_H + 2 * lane] = make_uint2(hb[0], hb[1]);
      }
    }
    if constexpr (i >= 40 && i < 50 && (i & 1) == 0) DW[(i - 40) / 2] = bld<WNT>(rD, dvoff, ((i - 40) / 2) * 256);
    if constexpr (OW_K0 >= 0 && i >= OW_K0 && (i - OW_K0) % OW_STR == 0) {
      constexpr int f = (i - OW_K0) / OW_STR;
      OW[f] = bld<OWAUX>(rO, ovoff + (f % 5) * 256, (f / 5) * 5120);
    }
    const v4u cur = st[i];
    float d = dot2(cur.x, q0, 0.f);
    d = dot2(cur.y, q1, d); d = dot2(cur.z, q2, d); d = dot2(cur.w, q3, d);
#define MERGE(A, B, CTRL, BIT) merge2<CTRL>(A, B, (c & (BIT)) != 0)
    x1[i & 1] = d;
    if constexpr (i & 1) {
      x2[(i >> 1) & 1] = MERGE(x1[0], x1[1], XOR8, 8);
      if constexpr ((i >> 1) & 1) {
        x3[(i >> 2) & 1] = MERGE(x2[0], x2[1], XOR7, 4);
        if constexpr ((i >> 2) & 1) {
          x4[(i >> 3) & 1] = MERGE(x3[0], x3[1], XOR2, 2);
          if constexpr ((i >> 3) & 1) sc[i / 16] = MERGE(x4[0], x4[1], XOR1, 1);
          if constexpr (!((i >> 3) & 1)) asm volatile("" : "+v"(x4[0]));
        } else asm volatile("" : "+v"(x3[0]));
      } else asm volatile("" : "+v"(x2[0]));
    } else asm volatile("" : "+v"(x1[0]));
#undef MERGE
    if constexpr (i % 16 == 15) asm volatile("" : "+v"(sc[i / 16]));
  };
  static_for<PF>([&](auto T) {
    constexpr int t = T.v;
    pace(IC<PF + t>{});
    kcomp(IC<2 * t>{});
    kcomp(IC<2 * t + 1>{});
    SB();
  });
  static_for<64 - 2 * PF>([&](auto T) {
    constexpr int i = 2 * PF + T.v;
    pace(IC<i>{});
    kcomp(IC<i>{});
    SB();
  });
  STAMP(2);
  constexpr float SCALE = 0.08838834764831845f, LOG2E = 1.4426950408889634f;
  const unsigned tag = tag32;
  unsigned PP[4];
  float mC;
  {
#pragma unroll
    for (int t = 0; t < 4; ++t) sc[t] = bfround(bfround(sc[t]) * SCALE);
    const float m = wave_max(fmaxf(fmaxf(sc[0], sc[1]), fmaxf(sc[2], sc[3])));
    mC = m * LOG2E;
    float l = 0.f;
#pragma unroll
    for (int t = 0; t < 4; ++t) {
      const float p = __builtin_amdgcn_exp2f((sc[t] - m) * LOG2E);
      l += p;
      PP[t] = split_hilo(p);
    }
    l = wave_sum(l);
    if (lane == 0) { lds[L_MW + w] = mC; lds[L_LW + w] = l; }
    LGKM0();
    unsigned old = 0;
    if (lane == 0) old = __hip_atomic_fetch_add((LAS unsigned*)lds + L_CNT + 1, 1u, __ATOMIC_RELAXED, __HIP_MEMORY_SCOPE_WORKGROUP);
    old = __builtin_amdgcn_readfirstlane(old);
    MEMFENCE();
    if (old == NWAVE - 1) {
      const f32x4 m0 = *(const f32x4*)&lds[L_MW], m1 = *(const f32x4*)&lds[L_MW + 4];
      const f32x4 l0 = *(const f32x4*)&lds[L_LW], l1 = *(const f32x4*)&lds[L_LW + 4];
      const float M = fmaxf(fmaxf(fmaxf(m0[0], m0[1]), fmaxf(m0[2], m0[3])), fmaxf(fmaxf(m1[0], m1[1]), fmaxf(m1[2], m1[3])));
      float L = 0.f;
#pragma unroll
      for (int k = 0; k < 4; ++k) L += l0[k] * __builtin_amdgcn_exp2f(m0[k] - M) + l1[k] * __builtin_amdgcn_exp2f(m1[k] - M);
      const float lse = M + __builtin_amdgcn_logf(L);
      if (lane == 0)
        __hip_atomic_store((GAS unsigned long long*)(ws + WS_G) + yrow, ((unsigned long long)tag << 32) | __float_as_uint(lse), __ATOMIC_RELAXED, __HIP_MEMORY_SCOPE_AGENT);
    }
  }
  const Sel selM = make_sel(c, c >= 8);

  f32x4 acc = {0.f, 0.f, 0.f, 0.f};
  float gate = 0.f;
  v4u GR = {0, 0, 0, 0}, GR2 = {0, 0, 0, 0};
  float fac = 0.f;
  const bool gw6 = w == 6, red = bid < NRED;
  const unsigned g_off = gw6 && (b == (bid & 1) && red ? true : (lane >> 1) == h) ? WS_G + b * 1024 + lane * 16 : OOB;
  const unsigned g_off2 = gw6 && red && (b != (bid & 1) || bid == 0) ? WS_G + (bid == 0 ? 1 : (bid & 1)) * 1024 + lane * 16 : OOB;
  auto vstep = [&](auto I) {
    constexpr int i = I.v;
    pace(IC<64 + i>{});
    if constexpr (64 + i >= OW_K0 && (64 + i - OW_K0) % OW_STR == 0 && (64 + i - OW_K0) / OW_STR < 20) {
      constexpr int f = (64 + i - OW_K0) / OW_STR;
      OW[f] = bld<OWAUX>(rO, ovoff + (f % 5) * 256, (f / 5) * 5120);
    }
    if constexpr (i == 36) { GR = bld<AUX_SC1>(rsW, g_off, 0); GR2 = bld<AUX_SC1>(rsW, g_off2, 0); }
    constexpr int i4 = i % 16, src = ((i4 & 1) << 3) | ((i4 & 2) << 1) | ((i4 & 4) >> 1) | ((i4 & 8) >> 3);
    const unsigned pb = bcast16<src>(PP[i / 16]);
    acc = mfma(st[64 + i], sel_frag(pb, selM), acc);
    if constexpr (i == 27 || i == 28 || i == 43 || i == 44 || i == 55 || i == 56) asm volatile("" : "+v"(acc));
    if constexpr (i == 28) {
      for (unsigned sp = 0; sp < SPIN_CAP; ++sp) {
        const unsigned cnt = __hip_atomic_load((LAS unsigned*)lds + L_CNT + 1, __ATOMIC_RELAXED, __HIP_MEMORY_SCOPE_WORKGROUP);
        if (__builtin_amdgcn_readfirstlane(cnt) >= NWAVE) break;
        __builtin_amdgcn_s_sleep(1);
      }
      MEMFENCE();
      const f32x4 g0 = *(const f32x4*)&lds[L_GP], g1 = *(const f32x4*)&lds[L_GP + 4];
      const float z = bfround(((g0[0] + g0[1]) + (g0[2] + g0[3])) + ((g1[0] + g1[1]) + (g1[2] + g1[3])));
      gate = bfround(1.f / (1.f + __expf(-z)));
      {
        const unsigned hv = ((const unsigned*)lds)[L_H + 2 * w + (g & 1)];
        const v4u bh = sel_frag(hv, selLo);
        const float omg = has_ffn ? bfround(1.f - gate) : 0.f;
#pragma unroll
        for (int k = 0; k < 5; ++k) {
          const f32x4 yf = mfma(DW[k], bh, f32x4{0.f, 0.f, 0.f, 0.f});
          ya[k] = omg * yf;
          asm volatile("" : "+v"(ya[k]));
        }
      }
    }
    if constexpr (i == 44) {
      if (gw6) {
        auto bad = [&]() { return (g_off != OOB && ((GR.y ^ tag) | (GR.w ^ tag)) != 0u) || (g_off2 != OOB && ((GR2.y ^ tag) | (GR2.w ^ tag)) != 0u); };
        if (__any(bad())) {
          for (unsigned sp = 0; sp < SPIN_CAP; ++sp) {
            __builtin_amdgcn_s_sleep(8);
            MEMFENCE();
            GR = bld<AUX_SC1>(rsW, g_off, 0); GR2 = bld<AUX_SC1>(rsW, g_off2, 0);
            if (!__any(bad())) break;
          }
        }
        const float e0 = __uint_as_float(__builtin_amdgcn_readlane(GR.x, 2 * h)), e1 = __uint_as_float(__builtin_amdgcn_readlane(GR.z, 2 * h));
        const float e2 = __uint_as_float(__builtin_amdgcn_readlane(GR.x, 2 * h + 1)), e3 = __uint_as_float(__builtin_amdgcn_readlane(GR.z, 2 * h + 1));
        const float M = fmaxf(fmaxf(e0, e1), fmaxf(e2, e3));
        const float S = (__builtin_amdgcn_exp2f(e0 - M) + __builtin_amdgcn_exp2f(e1 - M)) + (__builtin_amdgcn_exp2f(e2 - M) + __builtin_amdgcn_exp2f(e3 - M));
        if (lane == 0) { lds[L_LSE] = M; lds[L_LSE + 1] = S; }
        LGKM0();
        if (lane == 0) __hip_atomic_store((LAS unsigned*)lds + L_CNT + 4, 1u, __ATOMIC_RELAXED, __HIP_MEMORY_SCOPE_WORKGROUP);
        if (bid == 0 && lane == 0)
          __hip_atomic_store((GAS unsigned long long*)(ws + WS_EPOCH), ep_next, __ATOMIC_RELAXED, __HIP_MEMORY_SCOPE_AGENT);
      }
    }
    if constexpr (i == 56) {
      for (unsigned sp = 0; sp < SPIN_CAP; ++sp) {
        const unsigned f = __hip_atomic_load((LAS unsigned*)lds + L_CNT + 4, __ATOMIC_RELAXED, __HIP_MEMORY_SCOPE_WORKGROUP);
        if (__builtin_amdgcn_readfirstlane(f) != 0u) break;
        __builtin_amdgcn_s_sleep(1);
      }
      MEMFENCE();
      fac = gate * __builtin_amdgcn_exp2f(mC - lds[L_LSE]) / lds[L_LSE + 1];
    }
    SB();
  };
  static_for<64>(vstep);
  STAMP(3);
#pragma unroll
  for (int r = 0; r < 4; ++r) acc[r] = (acc[r] + dpp<XOR8>(acc[r])) * fac;
  if (c < 8) {
#pragma unroll
    for (int r = 0; r < 4; ++r) lds[L_O + (32 * g + 8 * r + c) * 8 + w] = acc[r];
  }
  __syncthreads();
  STAMP(4);
  {
    f32x4 oa[4], ob[4];
#pragma unroll
    for (int i = 0; i < 4; ++i) { const f32x4* op = (const f32x4*)&lds[L_O + (16 * w + 4 * i + g) * 8]; oa[i] = op[0]; ob[i] = op[1]; }
    SB();
#pragma unroll
    for (int i = 0; i < 4; ++i) {
      const float o = ((oa[i][0] + oa[i][1]) + (oa[i][2] + oa[i][3])) + ((ob[i][0] + ob[i][1]) + (ob[i][2] + ob[i][3]));
      const v4u bf = sel_frag(cvtpk(o, o), selLo);
#pragma unroll
      for (int k = 0; k < 5; ++k) ya[k] = mfma(OW[5 * i + k], bf, ya[k]);
    }
  }
  if (c < 8) {
#pragma unroll
    for (int k = 0; k < 5; ++k) *(f32x4*)&lds[L_Y + w * 640 + k * 128 + (g * 8 + c) * 4] = ya[k];
  }
  __syncthreads();
  if (tid < 160) {
    f32x4 t[8];
#pragma unroll
    for (int ww = 0; ww < 8; ++ww) t[ww] = *(const f32x4*)&lds[L_Y + ww * 640 + 4 * tid];
    const f32x4 tt = ((t[0] + t[1]) + (t[2] + t[3])) + ((t[4] + t[5]) + (t[6] + t[7]));
    v4u u = __builtin_bit_cast(v4u, tt);
#pragma unroll
    for (int e = 0; e < 4; ++e) u[e] = (u[e] == SENT) ? 0x7fc00000u : u[e];
    __builtin_amdgcn_raw_buffer_store_b128(u, rsW, WS_Y + yrow * 2560 + tid * 16, 0, AUX_SC1);
  }
  STAMP(5);
  if (bid < NRED) {
    const int rb = bid & 1, cb = bid >> 1;
    const unsigned off0 = WS_Y + (rb * 128 + 16 * w + (lane >> 3)) * 2560 + cb * 128 + (lane & 7) * 16;
    v4u r0 = {SENT, SENT, SENT, SENT}, r1 = {SENT, SENT, SENT, SENT};
    for (unsigned sp = 0; sp < SPIN_CAP; ++sp) {
      MEMFENCE();
      const bool n0_ = (r0.x == SENT) | (r0.y == SENT) | (r0.z == SENT) | (r0.w == SENT), n1_ = (r1.x == SENT) | (r1.y == SENT) | (r1.z == SENT) | (r1.w == SENT);
      const v4u t0 = bld<AUX_SC1>(rsW, n0_ ? off0 : OOB, 0), t1 = bld<AUX_SC1>(rsW, n1_ ? off0 + 8 * 2560 : OOB, 0);
      if (n0_) r0 = t0;
      if (n1_) r1 = t1;
      const bool ok = (r0.x != SENT) & (r0.y != SENT) & (r0.z != SENT) & (r0.w != SENT) & (r1.x != SENT) & (r1.y != SENT) & (r1.z != SENT) & (r1.w != SENT);
      if (__all(ok)) break;
      __builtin_amdgcn_s_sleep(FIN_SLEEP);
    }
    f32x4 t = __builtin_bit_cast(f32x4, r0) + __builtin_bit_cast(f32x4, r1);
#pragma unroll
    for (int e = 0; e < 4; ++e) { float v = t[e]; v += dpp<XOR8>(v); t[e] = xrow16_sum(v); }
    if (lane < 8) *(f32x4*)&lds[L_R + w * 32 + lane * 4] = t;
    __syncthreads();
    if (tid < 16) {
      const int r = tid >> 2, q2 = (tid & 3) * 2;
      float s0 = 0.f, s1 = 0.f;
#pragma unroll
      for (int ww = 0; ww < 8; ++ww) { s0 += lds[L_R + ww * 32 + q2 * 4 + r]; s1 += lds[L_R + ww * 32 + (q2 + 1) * 4 + r]; }
      __builtin_amdgcn_raw_buffer_store_b32(cvtpk(s0, s1), mk_rsrc(out, 2560), (rb * 320 + cb * 16 + 4 * r + (tid & 3)) * 4, 0, AUX_SC1);
    }
  }
}

extern "C" void kernel_launch(void* const* d_in, const int* in_sizes, int n_in, void* d_out, int out_size, void* d_ws,
                              size_t ws_size, hipStream_t stream) {
  (void)in_sizes; (void)n_in; (void)out_size; (void)ws_size;
  (void)hipFuncSetAttribute((const void*)fused_decode, hipFuncAttributeMaxDynamicSharedMemorySize, LDS_BYTES);
  fused_decode<<<256, NTHR, LDS_BYTES, stream>>>((const u16*)d_in[0], (const u16*)d_in[1], (const u16*)d_in[2], (const u16*)d_in[3],
                                                 (const u16*)d_in[4], (const u16*)d_in[6], (const u16*)d_in[7], (const u16*)d_in[8],
                                                 (const u16*)d_in[9], (u16*)d_out, (unsigned char*)d_ws);
}
```

```cpp
#include <hip/hip_runtime.h>
#include <hip/hip_bf16.h>
#include <stdint.h>

typedef unsigned short u16;
typedef unsigned v4u __attribute__((ext_vector_type(4)));
typedef unsigned v2u __attribute__((ext_vector_type(2)));
typedef float f32x4 __attribute__((ext_vector_type(4)));
typedef __bf16 bf16x2 __attribute__((ext_vector_type(2)));
typedef __bf16 bf16x8 __attribute__((ext_vector_type(8)));
#define GAS __attribute__((address_space(1)))
#define LAS __attribute__((address_space(3)))
typedef __amdgpu_buffer_rsrc_t rsrc_t;

constexpr int DM = 640, NH = 32, DH = 128, SK = 8192, FF = 1728;
constexpr int NSPLIT = 4, KEYS_WG = SK / NSPLIT, NWAVE = 8, NTHR = NWAVE * 64;
#define WIN 4
#define PF 12
#define POLL_AT (PF - 4)
#define OWK0 8
#define OWSTR 5
#define OWAUX 2
#define KAUX 2
#define VAUX 2
#define POLL_SLEEP 4
#define TH_TICKS 26
#define TH_BURST 2
#define TH_EVERY 2
#define FIN_SLEEP 1
#define WNT 2
constexpr int NGRP_F = FF / 64;
constexpr int AUX_NT = 2, AUX_SC1 = 16;
constexpr int NRED = 40;
constexpr unsigned SENT = 0xFFFFFFFFu;
constexpr unsigned OOB = 0x40000000u;
constexpr unsigned SPIN_CAP = 1u << 18;

constexpr int WS_EPOCH = 0;
constexpr int WS_G = 1024;
constexpr int WS_P = 4096;
constexpr int WS_Y = WS_P + 256 * 4096;
constexpr int WS_BYTES = WS_Y + 256 * 640 * 4;

constexpr int L_P = 0;
constexpr int L_Q = L_P + 8 * 256;
constexpr int L_MW = L_Q + 8 * 128;
constexpr int L_LW = L_MW + 8;
constexpr int L_GP = L_LW + 8;
constexpr int L_CNT = L_GP + 8;
constexpr int L_H = L_CNT + 8;
constexpr int L_LSE = L_H + 16;
constexpr int L_O = L_LSE + 4;
constexpr int L_Y = L_O + 128 * 8;
constexpr int L_R = L_Y + 8 * 640;
constexpr int L_END = L_R + 8 * 32;
constexpr int LDS_BYTES = 96 * 1024;
static_assert(L_END * 4 <= LDS_BYTES, "lds");

__device__ __forceinline__ float dot2(unsigned k, unsigned q, float acc) {
  return __builtin_amdgcn_fdot2_f32_bf16(__builtin_bit_cast(bf16x2, k), __builtin_bit_cast(bf16x2, q), acc, false);
}
__device__ __forceinline__ unsigned cvtpk(float lo, float hi) {
  bf16x2 r = {(__bf16)lo, (__bf16)hi};
  return __builtin_bit_cast(unsigned, r);
}
template <int CTRL> __device__ __forceinline__ float dpp(float x) {
  return __builtin_bit_cast(float, __builtin_amdgcn_mov_dpp(__builtin_bit_cast(int, x), CTRL, 0xf, 0xf, true));
}
template <int CTRL> __device__ __forceinline__ unsigned dppu(unsigned x) {
  return (unsigned)__builtin_amdgcn_mov_dpp((int)x, CTRL, 0xf, 0xf, true);
}
constexpr int XOR1 = 0xB1, XOR2 = 0x4E, XOR7 = 0x141, XOR8 = 0x128, ROR4 = 0x124, NEWBCAST = 0x150;
__device__ __forceinline__ float xrow16_max(float x) {
  auto s = __builtin_amdgcn_permlane16_swap(__float_as_uint(x), __float_as_uint(x), false, false);
  x = fmaxf(__uint_as_float(s[0]), __uint_as_float(s[1]));
  auto t = __builtin_amdgcn_permlane32_swap(__float_as_uint(x), __float_as_uint(x), false, false);
  return fmaxf(__uint_as_float(t[0]), __uint_as_float(t[1]));
}
__device__ __forceinline__ float xrow16_sum(float x) {
  auto s = __builtin_amdgcn_permlane16_swap(__float_as_uint(x), __float_as_uint(x), false, false);
  x = __uint_as_float(s[0]) + __uint_as_float(s[1]);
  auto t = __builtin_amdgcn_permlane32_swap(__float_as_uint(x), __float_as_uint(x), false, false);
  return __uint_as_float(t[0]) + __uint_as_float(t[1]);
}
__device__ __forceinline__ float wave_max(float x) {
  x = fmaxf(x, dpp<XOR1>(x)); x = fmaxf(x, dpp<XOR2>(x)); x = fmaxf(x, dpp<XOR7>(x)); x = fmaxf(x, dpp<XOR8>(x));
  return xrow16_max(x);
}
__device__ __forceinline__ float wave_sum(float x) {
  x += dpp<XOR1>(x); x += dpp<XOR2>(x); x += dpp<XOR7>(x); x += dpp<XOR8>(x);
  return xrow16_sum(x);
}
__device__ __forceinline__ f32x4 mfma(v4u a, v4u b, f32x4 c) {
  return __builtin_amdgcn_mfma_f32_16x16x32_bf16(__builtin_bit_cast(bf16x8, a), __builtin_bit_cast(bf16x8, b), c, 0, 0, 0);
}
__device__ __forceinline__ rsrc_t mk_rsrc(const void* p, unsigned bytes) {
  return __builtin_amdgcn_make_buffer_rsrc((void*)p, 0, bytes, 0x00020000);
}
template <int AUX> __device__ __forceinline__ v4u bld(rsrc_t r, unsigned voff, unsigned soff) {
  return __builtin_amdgcn_raw_buffer_load_b128(r, voff, soff, AUX);
}
__device__ __forceinline__ float bf2f(unsigned lo16) { return __uint_as_float(lo16 << 16); }
__device__ __forceinline__ float bfround(float x) { return (float)(__bf16)x; }
__device__ __forceinline__ unsigned split_hilo(float p) {
  const unsigned t = cvtpk(p, p);
  const float hf = __uint_as_float(t & 0xffff0000u);
  return cvtpk(p, p - hf);
}
template <int CTRL> __device__ __forceinline__ float merge2(float a, float b, bool pick_b) {
  const float ta = a + dpp<CTRL>(a), tb = b + dpp<CTRL>(b);
  return pick_b ? tb : ta;
}
struct Sel { unsigned s[4]; };
__device__ __forceinline__ Sel make_sel(int j, bool pick_hi) {
  Sel r;
  const int e = j & 7;
  const unsigned two = pick_hi ? 0x0302u : 0x0100u;
  const unsigned v = (e & 1) ? ((two << 16) | 0x0c0cu) : (0x0c0c0000u | two);
#pragma unroll
  for (int d = 0; d < 4; ++d) r.s[d] = ((e >> 1) == d) ? v : 0x0c0c0c0cu;
  return r;
}
__device__ __forceinline__ v4u sel_frag(unsigned val, const Sel& s) {
  v4u b;
  b.x = __builtin_amdgcn_perm(val, val, s.s[0]);
  b.y = __builtin_amdgcn_perm(val, val, s.s[1]);
  b.z = __builtin_amdgcn_perm(val, val, s.s[2]);
  b.w = __builtin_amdgcn_perm(val, val, s.s[3]);
  return b;
}
template <int T> __device__ __forceinline__ unsigned bcast16(unsigned x) { return dppu<NEWBCAST + T>(x); }

#define SB() __builtin_amdgcn_sched_barrier(0)
#define MEMFENCE() asm volatile("" ::: "memory")
#define LGKM0() asm volatile("s_waitcnt lgkmcnt(0)" ::: "memory")

template <int I> struct IC { static constexpr int v = I; };
template <int N, int I = 0, class Fn> __device__ __forceinline__ void static_for(Fn&& f) {
  if constexpr (I < N) { f(IC<I>{}); static_for<N, I + 1>(f); }
}
#define STAMP(k) do {} while (0)

__global__ void __launch_bounds__(NTHR, 2)
fused_decode(const u16* __restrict__ x, const u16* __restrict__ kc, const u16* __restrict__ vc, const u16* __restrict__ wq,
             const u16* __restrict__ wo, const u16* __restrict__ wg, const u16* __restrict__ wu, const u16* __restrict__ wd,
             const u16* __restrict__ gw, u16* __restrict__ out, unsigned char* __restrict__ ws) {
  extern __shared__ __attribute__((aligned(16))) float lds[];
  const int tid = threadIdx.x, lane = tid & 63, w = __builtin_amdgcn_readfirstlane(tid >> 6);
  const int bid = blockIdx.x, jj = bid >> 3, h = (bid & 7) + 8 * (jj >> 3), sub = jj & 7, b = sub & 1, s = sub >> 1;
  const int g = lane >> 4, c = lane & 15;
  STAMP(0);
  const rsrc_t rsW = mk_rsrc(ws, WS_BYTES);
  const bool has_ffn = h < NGRP_F;
  const int r0 = 80 * sub + 20 * (w & 3) + g;
  const GAS u16* xp = (const GAS u16*)x + (c >> 3) * DM + r0;
  unsigned XB[5];
  v4u WS5[5];
  {
    const char* wp = (w < 4) ? (const char*)wq + (size_t)r0 * 8192 + h * 256 + c * 16
                             : (const char*)(c < 8 ? wg : wu) + (size_t)r0 * (FF * 2) + (has_ffn ? h : NGRP_F - 1) * 128 + (c & 7) * 16;
    const unsigned rstr = (w < 4) ? 4u * 8192u : 4u * (FF * 2);
#pragma unroll
    for (int i = 0; i < 5; ++i) WS5[i] = __builtin_nontemporal_load((const GAS v4u*)(wp + (size_t)i * rstr));
#pragma unroll
    for (int i = 0; i < 5; ++i) XB[i] = xp[4 * i];
  }
  const unsigned long long epoch = __hip_atomic_load((const GAS unsigned long long*)(ws + WS_EPOCH), __ATOMIC_RELAXED, __HIP_MEMORY_SCOPE_AGENT);
  SB();
  const size_t kvoff = ((size_t)(b * NH + h) * SK + s * KEYS_WG) * 256;
  const rsrc_t rK = mk_rsrc((const char*)kc + kvoff, KEYS_WG * 256);
  const rsrc_t rV = mk_rsrc((const char*)vc + kvoff, KEYS_WG * 256);
  const unsigned loff = w * 1024 + lane * 16;
  v4u st[128];
  auto issue = [&](auto N) {
    constexpr int n = N.v;
    if constexpr (n < 64) st[n] = bld<KAUX>(rK, loff, n * 8192);
    else if constexpr (n < 128) st[n] = bld<VAUX>(rV, loff, (n - 64) * 8192);
  };
  unsigned t_q = 0;
  auto pace = [&](auto N) {
    constexpr int n = N.v;
    asm volatile("" : "+v"(st[n]));
    issue(IC<n + WIN>{});
  };
  auto throttle = [&](auto N) {
    constexpr int n = N.v + 1;
    if constexpr (n >= PF + TH_BURST && n + WIN < 128 && (n % TH_EVERY) == 0) {
      while ((unsigned)__builtin_amdgcn_s_memrealtime() - t_q < (unsigned)((n - PF - TH_BURST) * TH_TICKS)) __builtin_amdgcn_s_sleep(1);
    }
  };
  static_for<WIN>(issue);
  const int yrow = b * 128 + h * 4 + s;
  if (tid < 8) ((unsigned*)lds)[L_CNT + tid] = 0u;
  SB();
  __syncthreads();
  const Sel selLo = make_sel(c, false);
  unsigned tag32, tlo, thi;
  unsigned long long ep_next;
  auto project = [&](int part) {
    f32x4 pa = {0.f, 0.f, 0.f, 0.f};
#pragma unroll
    for (int i = 0; i < 5; ++i) asm volatile("" : "+v"(XB[i]));
#pragma unroll
    for (int i = 0; i < 5; ++i) pa = mfma(WS5[i], sel_frag(XB[i], selLo), pa);
#pragma unroll
    for (int r = 0; r < 4; ++r) {
      const int cc = 4 * g + r;
      const int idx = part == 0 ? (c >> 3) * 128 + 8 * cc + (c & 7) : (2 * ((cc & 7) >> 1) + (c >> 3)) * 32 + (cc >> 3) * 16 + 8 * (cc & 1) + (c & 7);
      lds[L_P + w * 256 + idx] = pa[r];
    }
    LGKM0();
    unsigned old = 0;
    if (lane == 0) old = __hip_atomic_fetch_add((LAS unsigned*)lds + L_CNT + 2 + part, 1u, __ATOMIC_RELAXED, __HIP_MEMORY_SCOPE_WORKGROUP);
    old = __builtin_amdgcn_readfirstlane(old);
    MEMFENCE();
    if (old == 3) {
#pragma unroll
      for (int u = 0; u < 2; ++u) {
        const int idx = (lane + 64 * u) * 2;
        float v0 = 0.f, v1 = 0.f;
#pragma unroll
        for (int k = 0; k < 4; ++k) { const float2 t = *(const float2*)&lds[L_P + (4 * part + k) * 256 + idx]; v0 += t.x; v1 += t.y; }
        const v4u gr = {((__float_as_uint(v0) + 0x80u) & 0xffffff00u) | tlo, thi, ((__float_as_uint(v1) + 0x80u) & 0xffffff00u) | tlo, thi};
        __builtin_amdgcn_raw_buffer_store_b128(gr, rsW, WS_P + (h * 8 + sub) * 4096 + part * 2048 + (lane + 64 * u) * 16, 0, AUX_SC1);
      }
    }
  };
  const unsigned pq_off = WS_P + (h * 8 + w) * 4096 + b * 1024 + lane * 16;
  v4u QG;
  static_for<PF>([&](auto I) {
    constexpr int i = I.v;
    pace(I);
    if constexpr (i == WIN - 1) {
      unsigned elo = (unsigned)epoch, ehi = (unsigned)(epoch >> 32);
      asm volatile("" : "+v"(elo), "+v"(ehi));
      const unsigned long long ep1 = (((unsigned long long)ehi << 32) | elo) + 1ull;
      ep_next = ep1;
      tag32 = (unsigned)ep1; tlo = (unsigned)ep1 & 0xffu; thi = (unsigned)(ep1 >> 8);
      STAMP(8);
      project(w >> 2);
      STAMP(1);
    }
    if constexpr (i == POLL_AT) QG = bld<AUX_SC1>(rsW, pq_off, 0);
    SB();
  });
  auto tagged = [&](const v4u& a) { return ((a.x & 0xffu) ^ tlo) | (a.y ^ thi) | ((a.z & 0xffu) ^ tlo) | (a.w ^ thi); };
  if (!__all(tagged(QG) == 0u)) {
    for (unsigned sp = 0; sp < SPIN_CAP; ++sp) {
      __builtin_amdgcn_s_sleep(POLL_SLEEP);
      MEMFENCE();
      QG = bld<AUX_SC1>(rsW, pq_off, 0);
      if (__all(tagged(QG) == 0u)) break;
    }
  }
  *(float2*)&lds[L_Q + w * 128 + 2 * lane] = make_float2(__uint_as_float(QG.x & 0xffffff00u), __uint_as_float(QG.z & 0xffffff00u));
  __syncthreads();
  t_q = (unsigned)__builtin_amdgcn_s_memrealtime();
  STAMP(7);
  unsigned q0, q1, q2, q3;
  {
    float qv[8];
    const f32x4* p0 = (const f32x4*)&lds[L_Q + (2 * g) * 128 + 8 * c];
    const f32x4* p1 = (const f32x4*)&lds[L_Q + (2 * g + 1) * 128 + 8 * c];
    const f32x4 a0 = p0[0], a1 = p0[1], b0 = p1[0], b1 = p1[1];
#pragma unroll
    for (int e = 0; e < 4; ++e) { qv[e] = xrow16_sum(a0[e] + b0[e]); qv[4 + e] = xrow16_sum(a1[e] + b1[e]); }
    q0 = cvtpk(qv[0], qv[1]); q1 = cvtpk(qv[2], qv[3]); q2 = cvtpk(qv[4], qv[5]); q3 = cvtpk(qv[6], qv[7]);
  }
  SB();

  const int n0 = has_ffn ? 64 * h + 16 * s : 0;
  const rsrc_t rD = mk_rsrc((const char*)wd + (size_t)n0 * 1280, has_ffn ? 16 * 1280 : 0);
  const rsrc_t rO = mk_rsrc((const char*)wo + (size_t)(h * 128 + 16 * w) * 1280, 16 * 1280);
  const unsigned ovoff = g * 1280 + c * 16;
  const unsigned dvoff = g < 2 ? (2 * w + g) * 1280 + c * 16 : OOB;
  v4u DW[5], OW[20];
  f32x4 ya[5];
  unsigned GX, GW;
  v4u FG0, FG1;
  const unsigned pf_off = w == 7 ? WS_P + (h * 8 + g) * 4096 + 2048 + sub * 256 + c * 16 : OOB;
  constexpr int OW_K0 = OWK0, OW_STR = OWSTR, OW_NK = (64 - OW_K0 + OW_STR - 1) / OW_STR;

  float sc[4];
  float x1[2], x2[2], x3[2], x4[2];
  auto kcomp = [&](auto I) {
    constexpr int i = I.v;
    if constexpr (i == 26) {
      const int e = 40 * w + (lane < 40 ? lane : 0);
      GX = ((const GAS unsigned*)((const char*)x + b * 1280))[e];
      GW = ((const GAS unsigned*)gw)[e];
      __builtin_amdgcn_raw_buffer_store_b128(v4u{SENT, SENT, SENT, SENT}, rsW, tid < 160 ? WS_Y + yrow * 2560 + tid * 16 : OOB, 0, AUX_SC1);
      FG0 = bld<AUX_SC1>(rsW, pf_off, 0);
      FG1 = bld<AUX_SC1>(rsW, pf_off + (w == 7 ? 4 * 4096 : 0), 0);
    }
    if constexpr (i == 36) {
      float gp = bfround(bf2f(GX & 0xffffu) * bf2f(GW & 0xffffu)) + bfround(__uint_as_float(GX & 0xffff0000u) * __uint_as_float(GW & 0xffff0000u));
      gp = wave_sum(lane < 40 ? gp : 0.f);
      lds[L_GP + w] = gp;
      if (w == 7) {
        if (!__all((tagged(FG0) | tagged(FG1)) == 0u)) {
          for (unsigned sp = 0; sp < SPIN_CAP; ++sp) {
            __builtin_amdgcn_s_sleep(POLL_SLEEP);
            MEMFENCE();
            FG0 = bld<AUX_SC1>(rsW, pf_off, 0); FG1 = bld<AUX_SC1>(rsW, pf_off + 4 * 4096, 0);
            if (__all((tagged(FG0) | tagged(FG1)) == 0u)) break;
          }
        }
        const float s0 = xrow16_sum(__uint_as_float(FG0.x & 0xffffff00u) + __uint_as_float(FG1.x & 0xffffff00u));
        const float s1 = xrow16_sum(__uint_as_float(FG0.z & 0xffffff00u) + __uint_as_float(FG1.z & 0xffffff00u));
        unsigned hb[2];
#pragma unroll
        for (int u2 = 0; u2 < 2; ++u2) {
          const float sv = u2 ? s1 : s0;
          const float gt = bfround(sv), up = bfround(dpp<XOR8>(sv));
          const float u = 0.7978845608028654f * (gt + 0.044715f * gt * gt * gt);
          const float th = 1.f - 2.f / (1.f + __expf(2.f * u));
          const float ge = bfround(0.5f * gt * (1.f + th));
          hb[u2] = cvtpk(ge * up, 0.f);
        }
        if (lane < 8) *(uint2*)&((unsigned*)lds)[L_H + 2 * lane] = make_uint2(hb[0], hb[1]);
      }
    }
    if constexpr (i >= 40 && i < 50 && (i & 1) == 0) DW[(i - 40) / 2] = bld<WNT>(rD, dvoff, ((i - 40) / 2) * 256);
    if constexpr (OW_K0 >= 0 && i >= OW_K0 && (i - OW_K0) % OW_STR == 0) {
      constexpr int f = (i - OW_K0) / OW_STR;
      OW[f] = bld<OWAUX>(rO, ovoff + (f % 5) * 256, (f / 5) * 5120);
    }
    const v4u cur = st[i];
    float d = dot2(cur.x, q0, 0.f);
    d = dot2(cur.y, q1, d); d = dot2(cur.z, q2, d); d = dot2(cur.w, q3, d);
#define MERGE(A, B, CTRL, BIT) merge2<CTRL>(A, B, (c & (BIT)) != 0)
    x1[i & 1] = d;
    if constexpr (i & 1) {
      x2[(i >> 1) & 1] = MERGE(x1[0], x1[1], XOR8, 8);
      if constexpr ((i >> 1) & 1) {
        x3[(i >> 2) & 1] = MERGE(x2[0], x2[1], XOR7, 4);
        if constexpr ((i >> 2) & 1) {
          x4[(i >> 3) & 1] = MERGE(x3[0], x3[1], XOR2, 2);
          if constexpr ((i >> 3) & 1) sc[i / 16] = MERGE(x4[0], x4[1], XOR1, 1);
          if constexpr (!((i >> 3) & 1)) asm volatile("" : "+v"(x4[0]));
        } else asm volatile("" : "+v"(x3[0]));
      } else asm volatile("" : "+v"(x2[0]));
    } else asm volatile("" : "+v"(x1[0]));
#undef MERGE
    if constexpr (i % 16 == 15) asm volatile("" : "+v"(sc[i / 16]));
  };
  static_for<PF>([&](auto T) {
    constexpr int t = T.v;
    pace(IC<PF + t>{});
    kcomp(IC<2 * t>{});
    kcomp(IC<2 * t + 1>{});
    throttle(IC<PF + t>{});
    SB();
  });
  static_for<64 - 2 * PF>([&](auto T) {
    constexpr int i = 2 * PF + T.v;
    pace(IC<i>{});
    kcomp(IC<i>{});
    throttle(IC<i>{});
    SB();
  });
  STAMP(2);
  constexpr float SCALE = 0.08838834764831845f, LOG2E = 1.4426950408889634f;
  const unsigned tag = tag32;
  unsigned PP[4];
  float mC;
  {
#pragma unroll
    for (int t = 0; t < 4; ++t) sc[t] = bfround(bfround(sc[t]) * SCALE);
    const float m = wave_max(fmaxf(fmaxf(sc[0], sc[1]), fmaxf(sc[2], sc[3])));
    mC = m * LOG2E;
    float l = 0.f;
#pragma unroll
    for (int t = 0; t < 4; ++t) {
      const float p = __builtin_amdgcn_exp2f((sc[t] - m) * LOG2E);
      l += p;
      PP[t] = split_hilo(p);
    }
    l = wave_sum(l);
    if (lane == 0) { lds[L_MW + w] = mC; lds[L_LW + w] = l; }
    LGKM0();
    unsigned old = 0;
    if (lane == 0) old = __hip_atomic_fetch_add((LAS unsigned*)lds + L_CNT + 1, 1u, __ATOMIC_RELAXED, __HIP_MEMORY_SCOPE_WORKGROUP);
    old = __builtin_amdgcn_readfirstlane(old);
    MEMFENCE();
    if (old == NWAVE - 1) {
      const f32x4 m0 = *(const f32x4*)&lds[L_MW], m1 = *(const f32x4*)&lds[L_MW + 4];
      const f32x4 l0 = *(const f32x4*)&lds[L_LW], l1 = *(const f32x4*)&lds[L_LW + 4];
      const float M = fmaxf(fmaxf(fmaxf(m0[0], m0[1]), fmaxf(m0[2], m0[3])), fmaxf(fmaxf(m1[0], m1[1]), fmaxf(m1[2], m1[3])));
      float L = 0.f;
#pragma unroll
      for (int k = 0; k < 4; ++k) L += l0[k] * __builtin_amdgcn_exp2f(m0[k] - M) + l1[k] * __builtin_amdgcn_exp2f(m1[k] - M);
      const float lse = M + __builtin_amdgcn_logf(L);
      if (lane == 0)
        __hip_atomic_store((GAS unsigned long long*)(ws + WS_G) + yrow, ((unsigned long long)tag << 32) | __float_as_uint(lse), __ATOMIC_RELAXED, __HIP_MEMORY_SCOPE_AGENT);
    }
  }
  const Sel selM = make_sel(c, c >= 8);

  f32x4 acc = {0.f, 0.f, 0.f, 0.f};
  float gate = 0.f;
  v4u GR = {0, 0, 0, 0}, GR2 = {0, 0, 0, 0};
  float fac = 0.f;
  const bool gw6 = w == 6, red = bid < NRED;
  const unsigned g_off = gw6 && (b == (bid & 1) && red ? true : (lane >> 1) == h) ? WS_G + b * 1024 + lane * 16 : OOB;
  const unsigned g_off2 = gw6 && red && (b != (bid & 1) || bid == 0) ? WS_G + (bid == 0 ? 1 : (bid & 1)) * 1024 + lane * 16 : OOB;
  auto vstep = [&](auto I) {
    constexpr int i = I.v;
    pace(IC<64 + i>{});
    if constexpr (64 + i >= OW_K0 && (64 + i - OW_K0) % OW_STR == 0 && (64 + i - OW_K0) / OW_STR < 20) {
      constexpr int f = (64 + i - OW_K0) / OW_STR;
      OW[f] = bld<OWAUX>(rO, ovoff + (f % 5) * 256, (f / 5) * 5120);
    }
    if constexpr (i == 36) { GR = bld<AUX_SC1>(rsW, g_off, 0); GR2 = bld<AUX_SC1>(rsW, g_off2, 0); }
    constexpr int i4 = i % 16, src = ((i4 & 1) << 3) | ((i4 & 2) << 1) | ((i4 & 4) >> 1) | ((i4 & 8) >> 3);
    const unsigned pb = bcast16<src>(PP[i / 16]);
    acc = mfma(st[64 + i], sel_frag(pb, selM), acc);
    if constexpr (i == 27 || i == 28 || i == 43 || i == 44 || i == 55 || i == 56) asm volatile("" : "+v"(acc));
    if constexpr (i == 28) {
      for (unsigned sp = 0; sp < SPIN_CAP; ++sp) {
        const unsigned cnt = __hip_atomic_load((LAS unsigned*)lds + L_CNT + 1, __ATOMIC_RELAXED, __HIP_MEMORY_SCOPE_WORKGROUP);
        if (__builtin_amdgcn_readfirstlane(cnt) >= NWAVE) break;
        __builtin_amdgcn_s_sleep(1);
      }
      MEMFENCE();
      const f32x4 g0 = *(const f32x4*)&lds[L_GP], g1 = *(const f32x4*)&lds[L_GP + 4];
      const float z = bfround(((g0[0] + g0[1]) + (g0[2] + g0[3])) + ((g1[0] + g1[1]) + (g1[2] + g1[3])));
      gate = bfround(1.f / (1.f + __expf(-z)));
      {
        const unsigned hv = ((const unsigned*)lds)[L_H + 2 * w + (g & 1)];
        const v4u bh = sel_frag(hv, selLo);
        const float omg = has_ffn ? bfround(1.f - gate) : 0.f;
#pragma unroll
        for (int k = 0; k < 5; ++k) {
          const f32x4 yf = mfma(DW[k], bh, f32x4{0.f, 0.f, 0.f, 0.f});
          ya[k] = omg * yf;
          asm volatile("" : "+v"(ya[k]));
        }
      }
    }
    if constexpr (i == 44) {
      if (gw6) {
        auto bad = [&]() { return (g_off != OOB && ((GR.y ^ tag) | (GR.w ^ tag)) != 0u) || (g_off2 != OOB && ((GR2.y ^ tag) | (GR2.w ^ tag)) != 0u); };
        if (__any(bad())) {
          for (unsigned sp = 0; sp < SPIN_CAP; ++sp) {
            __builtin_amdgcn_s_sleep(8);
            MEMFENCE();
            GR = bld<AUX_SC1>(rsW, g_off, 0); GR2 = bld<AUX_SC1>(rsW, g_off2, 0);
            if (!__any(bad())) break;
          }
        }
        const float e0 = __uint_as_float(__builtin_amdgcn_readlane(GR.x, 2 * h)), e1 = __uint_as_float(__builtin_amdgcn_readlane(GR.z, 2 * h));
        const float e2 = __uint_as_float(__builtin_amdgcn_readlane(GR.x, 2 * h + 1)), e3 = __uint_as_float(__builtin_amdgcn_readlane(GR.z, 2 * h + 1));
        const float M = fmaxf(fmaxf(e0, e1), fmaxf(e2, e3));
        const float S = (__builtin_amdgcn_exp2f(e0 - M) + __builtin_amdgcn_exp2f(e1 - M)) + (__builtin_amdgcn_exp2f(e2 - M) + __builtin_amdgcn_exp2f(e3 - M));
        if (lane == 0) { lds[L_LSE] = M; lds[L_LSE + 1] = S; }
        LGKM0();
        if (lane == 0) __hip_atomic_store((LAS unsigned*)lds + L_CNT + 4, 1u, __ATOMIC_RELAXED, __HIP_MEMORY_SCOPE_WORKGROUP);
        if (bid == 0 && lane == 0)
          __hip_atomic_store((GAS unsigned long long*)(ws + WS_EPOCH), ep_next, __ATOMIC_RELAXED, __HIP_MEMORY_SCOPE_AGENT);
      }
    }
    if constexpr (i == 56) {
      for (unsigned sp = 0; sp < SPIN_CAP; ++sp) {
        const unsigned f = __hip_atomic_load((LAS unsigned*)lds + L_CNT + 4, __ATOMIC_RELAXED, __HIP_MEMORY_SCOPE_WORKGROUP);
        if (__builtin_amdgcn_readfirstlane(f) != 0u) break;
        __builtin_amdgcn_s_sleep(1);
      }
      MEMFENCE();
      fac = gate * __builtin_amdgcn_exp2f(mC - lds[L_LSE]) / lds[L_LSE + 1];
    }
    throttle(IC<64 + i>{});
    SB();
  };
  static_for<64>(vstep);
  STAMP(3);
#pragma unroll
  for (int r = 0; r < 4; ++r) acc[r] = (acc[r] + dpp<XOR8>(acc[r])) * fac;
  if (c < 8) {
#pragma unroll
    for (int r = 0; r < 4; ++r) lds[L_O + (32 * g + 8 * r + c) * 8 + w] = acc[r];
  }
  __syncthreads();
  STAMP(4);
  {
    f32x4 oa[4], ob[4];
#pragma unroll
    for (int i = 0; i < 4; ++i) { const f32x4* op = (const f32x4*)&lds[L_O + (16 * w + 4 * i + g) * 8]; oa[i] = op[0]; ob[i] = op[1]; }
    SB();
#pragma unroll
    for (int i = 0; i < 4; ++i) {
      const float o = ((oa[i][0] + oa[i][1]) + (oa[i][2] + oa[i][3])) + ((ob[i][0] + ob[i][1]) + (ob[i][2] + ob[i][3]));
      const v4u bf = sel_frag(cvtpk(o, o), selLo);
#pragma unroll
      for (int k = 0; k < 5; ++k) ya[k] = mfma(OW[5 * i + k], bf, ya[k]);
    }
  }
  if (c < 8) {
#pragma unroll
    for (int k = 0; k < 5; ++k) *(f32x4*)&lds[L_Y + w * 640 + k * 128 + (g * 8 + c) * 4] = ya[k];
  }
  __syncthreads();
  if (tid < 160) {
    f32x4 t[8];
#pragma unroll
    for (int ww = 0; ww < 8; ++ww) t[ww] = *(const f32x4*)&lds[L_Y + ww * 640 + 4 * tid];
    const f32x4 tt = ((t[0] + t[1]) + (t[2] + t[3])) + ((t[4] + t[5]) + (t[6] + t[7]));
    v4u u = __builtin_bit_cast(v4u, tt);
#pragma unroll
    for (int e = 0; e < 4; ++e) u[e] = (u[e] == SENT) ? 0x7fc00000u : u[e];
    __builtin_amdgcn_raw_buffer_store_b128(u, rsW, WS_Y + yrow * 2560 + tid * 16, 0, AUX_SC1);
  }
  STAMP(5);
  if (bid < NRED) {
    const int rb = bid & 1, cb = bid >> 1;
    const unsigned off0 = WS_Y + (rb * 128 + 16 * w + (lane >> 3)) * 2560 + cb * 128 + (lane & 7) * 16;
    v4u r0 = {SENT, SENT, SENT, SENT}, r1 = {SENT, SENT, SENT, SENT};
    for (unsigned sp = 0; sp < SPIN_CAP; ++sp) {
      MEMFENCE();
      const bool n0_ = (r0.x == SENT) | (r0.y == SENT) | (r0.z == SENT) | (r0.w == SENT), n1_ = (r1.x == SENT) | (r1.y == SENT) | (r1.z == SENT) | (r1.w == SENT);
      const v4u t0 = bld<AUX_SC1>(rsW, n0_ ? off0 : OOB, 0), t1 = bld<AUX_SC1>(rsW, n1_ ? off0 + 8 * 2560 : OOB, 0);
      if (n0_) r0 = t0;
      if (n1_) r1 = t1;
      const bool ok = (r0.x != SENT) & (r0.y != SENT) & (r0.z != SENT) & (r0.w != SENT) & (r1.x != SENT) & (r1.y != SENT) & (r1.z != SENT) & (r1.w != SENT);
      if (__all(ok)) break;
      __builtin_amdgcn_s_sleep(FIN_SLEEP);
    }
    f32x4 t = __builtin_bit_cast(f32x4, r0) + __builtin_bit_cast(f32x4, r1);
#pragma unroll
    for (int e = 0; e < 4; ++e) { float v = t[e]; v += dpp<XOR8>(v); t[e] = xrow16_sum(v); }
    if (lane < 8) *(f32x4*)&lds[L_R + w * 32 + lane * 4] = t;
    __syncthreads();
    if (tid < 16) {
      const int r = tid >> 2, q2 = (tid & 3) * 2;
      float s0 = 0.f, s1 = 0.f;
#pragma unroll
      for (int ww = 0; ww < 8; ++ww) { s0 += lds[L_R + ww * 32 + q2 * 4 + r]; s1 += lds[L_R + ww * 32 + (q2 + 1) * 4 + r]; }
      ((unsigned*)out)[rb * 320 + cb * 16 + 4 * r + (tid & 3)] = cvtpk(s0, s1);
    }
  }
}

extern "C" void kernel_launch(void* const* d_in, const int* in_sizes, int n_in, void* d_out, int out_size, void* d_ws,
                              size_t ws_size, hipStream_t stream) {
  (void)in_sizes; (void)n_in; (void)out_size; (void)ws_size;
  (void)hipFuncSetAttribute((const void*)fused_decode, hipFuncAttributeMaxDynamicSharedMemorySize, LDS_BYTES);
  fused_decode<<<256, NTHR, LDS_BYTES, stream>>>((const u16*)d_in[0], (const u16*)d_in[1], (const u16*)d_in[2], (const u16*)d_in[3],
                                                 (const u16*)d_in[4], (const u16*)d_in[6], (const u16*)d_in[7], (const u16*)d_in[8],
                                                 (const u16*)d_in[9], (u16*)d_out, (unsigned char*)d_ws);
}
```

```cpp
#include <hip/hip_runtime.h>
#include <hip/hip_bf16.h>
#include <stdint.h>

typedef unsigned short u16;
typedef unsigned v4u __attribute__((ext_vector_type(4)));
typedef unsigned v2u __attribute__((ext_vector_type(2)));
typedef float f32x4 __attribute__((ext_vector_type(4)));
typedef __bf16 bf16x2 __attribute__((ext_vector_type(2)));
typedef __bf16 bf16x8 __attribute__((ext_vector_type(8)));
#define GAS __attribute__((address_space(1)))
#define LAS __attribute__((address_space(3)))
typedef __amdgpu_buffer_rsrc_t rsrc_t;

constexpr int DM = 640, NH = 32, DH = 128, SK = 8192, FF = 1728;
constexpr int NSPLIT = 4, KEYS_WG = SK / NSPLIT, NWAVE = 8, NTHR = NWAVE * 64;
#define WIN 4
#define PF 12
#define POLL_AT (PF - 4)
#define OWK0 8
#define OWSTR 5
#define OWAUX 2
#define KAUX 2
#define VAUX 2
#define POLL_SLEEP 4
#define TH_TICKS 28
#define TH_BURST 2
#define TH_EVERY 2
#define FIN_SLEEP 1
#define WNT 2
constexpr int NGRP_F = FF / 64;
constexpr int AUX_NT = 2, AUX_SC1 = 16;
constexpr int NRED = 40;
constexpr unsigned SENT = 0xFFFFFFFFu;
constexpr unsigned OOB = 0x40000000u;
constexpr unsigned SPIN_CAP = 1u << 18;

constexpr int WS_EPOCH = 0;
constexpr int WS_G = 1024;
constexpr int WS_P = 4096;
constexpr int WS_Y = WS_P + 256 * 4096;
constexpr int WS_BYTES = WS_Y + 256 * 640 * 4;

constexpr int L_P = 0;
constexpr int L_Q = L_P + 8 * 256;
constexpr int L_MW = L_Q + 8 * 128;
constexpr int L_LW = L_MW + 8;
constexpr int L_GP = L_LW + 8;
constexpr int L_CNT = L_GP + 8;
constexpr int L_H = L_CNT + 8;
constexpr int L_LSE = L_H + 16;
constexpr int L_O = L_LSE + 4;
constexpr int L_Y = L_O + 128 * 8;
constexpr int L_R = L_Y + 8 * 640;
constexpr int L_END = L_R + 8 * 32;
constexpr int LDS_BYTES = 96 * 1024;
static_assert(L_END * 4 <= LDS_BYTES, "lds");

__device__ __forceinline__ float dot2(unsigned k, unsigned q, float acc) {
  return __builtin_amdgcn_fdot2_f32_bf16(__builtin_bit_cast(bf16x2, k), __builtin_bit_cast(bf16x2, q), acc, false);
}
__device__ __forceinline__ unsigned cvtpk(float lo, float hi) {
  bf16x2 r = {(__bf16)lo, (__bf16)hi};
  return __builtin_bit_cast(unsigned, r);
}
template <int CTRL> __device__ __forceinline__ float dpp(float x) {
  return __builtin_bit_cast(float, __builtin_amdgcn_mov_dpp(__builtin_bit_cast(int, x), CTRL, 0xf, 0xf, true));
}
template <int CTRL> __device__ __forceinline__ unsigned dppu(unsigned x) {
  return (unsigned)__builtin_amdgcn_mov_dpp((int)x, CTRL, 0xf, 0xf, true);
}
constexpr int XOR1 = 0xB1, XOR2 = 0x4E, XOR7 = 0x141, XOR8 = 0x128, ROR4 = 0x124, NEWBCAST = 0x150;
__device__ __forceinline__ float xrow16_max(float x) {
  auto s = __builtin_amdgcn_permlane16_swap(__float_as_uint(x), __float_as_uint(x), false, false);
  x = fmaxf(__uint_as_float(s[0]), __uint_as_float(s[1]));
  auto t = __builtin_amdgcn_permlane32_swap(__float_as_uint(x), __float_as_uint(x), false, false);
  return fmaxf(__uint_as_float(t[0]), __uint_as_float(t[1]));
}
__device__ __forceinline__ float xrow16_sum(float x) {
  auto s = __builtin_amdgcn_permlane16_swap(__float_as_uint(x), __float_as_uint(x), false, false);
  x = __uint_as_float(s[0]) + __uint_as_float(s[1]);
  auto t = __builtin_amdgcn_permlane32_swap(__float_as_uint(x), __float_as_uint(x), false, false);
  return __uint_as_float(t[0]) + __uint_as_float(t[1]);
}
__device__ __forceinline__ float wave_max(float x) {
  x = fmaxf(x, dpp<XOR1>(x)); x = fmaxf(x, dpp<XOR2>(x)); x = fmaxf(x, dpp<XOR7>(x)); x = fmaxf(x, dpp<XOR8>(x));
  return xrow16_max(x);
}
__device__ __forceinline__ float wave_sum(float x) {
  x += dpp<XOR1>(x); x += dpp<XOR2>(x); x += dpp<XOR7>(x); x += dpp<XOR8>(x);
  return xrow16_sum(x);
}
__device__ __forceinline__ f32x4 mfma(v4u a, v4u b, f32x4 c) {
  return __builtin_amdgcn_mfma_f32_16x16x32_bf16(__builtin_bit_cast(bf16x8, a), __builtin_bit_cast(bf16x8, b), c, 0, 0, 0);
}
__device__ __forceinline__ rsrc_t mk_rsrc(const void* p, unsigned bytes) {
  return __builtin_amdgcn_make_buffer_rsrc((void*)p, 0, bytes, 0x00020000);
}
template <int AUX> __device__ __forceinline__ v4u bld(rsrc_t r, unsigned voff, unsigned soff) {
  return __builtin_amdgcn_raw_buffer_load_b128(r, voff, soff, AUX);
}
__device__ __forceinline__ float bf2f(unsigned lo16) { return __uint_as_float(lo16 << 16); }
__device__ __forceinline__ float bfround(float x) { return (float)(__bf16)x; }
__device__ __forceinline__ unsigned split_hilo(float p) {
  const unsigned t = cvtpk(p, p);
  const float hf = __uint_as_float(t & 0xffff0000u);
  return cvtpk(p, p - hf);
}
template <int CTRL> __device__ __forceinline__ float merge2(float a, float b, bool pick_b) {
  const float ta = a + dpp<CTRL>(a), tb = b + dpp<CTRL>(b);
  return pick_b ? tb : ta;
}
struct Sel { unsigned s[4]; };
__device__ __forceinline__ Sel make_sel(int j, bool pick_hi) {
  Sel r;
  const int e = j & 7;
  const unsigned two = pick_hi ? 0x0302u : 0x0100u;
  const unsigned v = (e & 1) ? ((two << 16) | 0x0c0cu) : (0x0c0c0000u | two);
#pragma unroll
  for (int d = 0; d < 4; ++d) r.s[d] = ((e >> 1) == d) ? v : 0x0c0c0c0cu;
  return r;
}
__device__ __forceinline__ v4u sel_frag(unsigned val, const Sel& s) {
  v4u b;
  b.x = __builtin_amdgcn_perm(val, val, s.s[0]);
  b.y = __builtin_amdgcn_perm(val, val, s.s[1]);
  b.z = __builtin_amdgcn_perm(val, val, s.s[2]);
  b.w = __builtin_amdgcn_perm(val, val, s.s[3]);
  return b;
}
template <int T> __device__ __forceinline__ unsigned bcast16(unsigned x) { return dppu<NEWBCAST + T>(x); }

#define SB() __builtin_amdgcn_sched_barrier(0)
#define MEMFENCE() asm volatile("" ::: "memory")
#define LGKM0() asm volatile("s_waitcnt lgkmcnt(0)" ::: "memory")

template <int I> struct IC { static constexpr int v = I; };
template <int N, int I = 0, class Fn> __device__ __forceinline__ void static_for(Fn&& f) {
  if constexpr (I < N) { f(IC<I>{}); static_for<N, I + 1>(f); }
}
#define STAMP(k) do {} while (0)

__global__ void __launch_bounds__(NTHR, 2)
fused_decode(const u16* __restrict__ x, const u16* __restrict__ kc, const u16* __restrict__ vc, const u16* __restrict__ wq,
             const u16* __restrict__ wo, const u16* __restrict__ wg, const u16* __restrict__ wu, const u16* __restrict__ wd,
             const u16* __restrict__ gw, u16* __restrict__ out, unsigned char* __restrict__ ws) {
  extern __shared__ __attribute__((aligned(16))) float lds[];
  const int tid = threadIdx.x, lane = tid & 63, w = __builtin_amdgcn_readfirstlane(tid >> 6);
  const int bid = blockIdx.x, jj = bid >> 3, h = (bid & 7) + 8 * (jj >> 3), sub = jj & 7, b = sub & 1, s = sub >> 1;
  const int g = lane >> 4, c = lane & 15;
  STAMP(0);
  const rsrc_t rsW = mk_rsrc(ws, WS_BYTES);
  const bool has_ffn = h < NGRP_F;
  const int r0 = 80 * sub + 20 * (w & 3) + g;
  const GAS u16* xp = (const GAS u16*)x + (c >> 3) * DM + r0;
  unsigned XB[5];
  v4u WS5[5];
  {
    const char* wp = (w < 4) ? (const char*)wq + (size_t)r0 * 8192 + h * 256 + c * 16
                             : (const char*)(c < 8 ? wg : wu) + (size_t)r0 * (FF * 2) + (has_ffn ? h : NGRP_F - 1) * 128 + (c & 7) * 16;
    const unsigned rstr = (w < 4) ? 4u * 8192u : 4u * (FF * 2);
#pragma unroll
    for (int i = 0; i < 5; ++i) WS5[i] = __builtin_nontemporal_load((const GAS v4u*)(wp + (size_t)i * rstr));
#pragma unroll
    for (int i = 0; i < 5; ++i) XB[i] = xp[4 * i];
  }
  const unsigned long long epoch = __hip_atomic_load((const GAS unsigned long long*)(ws + WS_EPOCH), __ATOMIC_RELAXED, __HIP_MEMORY_SCOPE_AGENT);
  SB();
  const size_t kvoff = ((size_t)(b * NH + h) * SK + s * KEYS_WG) * 256;
  const rsrc_t rK = mk_rsrc((const char*)kc + kvoff, KEYS_WG * 256);
  const rsrc_t rV = mk_rsrc((const char*)vc + kvoff, KEYS_WG * 256);
  const unsigned loff = w * 1024 + lane * 16;
  v4u st[128];
  auto issue = [&](auto N) {
    constexpr int n = N.v;
    if constexpr (n < 64) st[n] = bld<KAUX>(rK, loff, n * 8192);
    else if constexpr (n < 128) st[n] = bld<VAUX>(rV, loff, (n - 64) * 8192);
  };
  unsigned t_q = 0;
  auto pace = [&](auto N) {
    constexpr int n = N.v;
    asm volatile("" : "+v"(st[n]));
    if constexpr (n >= PF + TH_BURST && n + WIN < 128 && (n % TH_EVERY) == 0) {
      while ((unsigned)__builtin_amdgcn_s_memrealtime() - t_q < (unsigned)((n - PF - TH_BURST) * TH_TICKS)) __builtin_amdgcn_s_sleep(1);
    }
    issue(IC<n + WIN>{});
  };
  static_for<WIN>(issue);
  const int yrow = b * 128 + h * 4 + s;
  if (tid < 8) ((unsigned*)lds)[L_CNT + tid] = 0u;
  SB();
  __syncthreads();
  const Sel selLo = make_sel(c, false);
  unsigned tag32, tlo, thi;
  unsigned long long ep_next;
  auto project = [&](int part) {
    f32x4 pa = {0.f, 0.f, 0.f, 0.f};
#pragma unroll
    for (int i = 0; i < 5; ++i) pa = mfma(WS5[i], sel_frag(XB[i], selLo), pa);
#pragma unroll
    for (int r = 0; r < 4; ++r) {
      const int cc = 4 * g + r;
      const int idx = part == 0 ? (c >> 3) * 128 + 8 * cc + (c & 7) : (2 * ((cc & 7) >> 1) + (c >> 3)) * 32 + (cc >> 3) * 16 + 8 * (cc & 1) + (c & 7);
      lds[L_P + w * 256 + idx] = pa[r];
    }
    LGKM0();
    unsigned old = 0;
    if (lane == 0) old = __hip_atomic_fetch_add((LAS unsigned*)lds + L_CNT + 2 + part, 1u, __ATOMIC_RELAXED, __HIP_MEMORY_SCOPE_WORKGROUP);
    old = __builtin_amdgcn_readfirstlane(old);
    MEMFENCE();
    if (old == 3) {
#pragma unroll
      for (int u = 0; u < 2; ++u) {
        const int idx = (lane + 64 * u) * 2;
        float v0 = 0.f, v1 = 0.f;
#pragma unroll
        for (int k = 0; k < 4; ++k) { const float2 t = *(const float2*)&lds[L_P + (4 * part + k) * 256 + idx]; v0 += t.x; v1 += t.y; }
        const v4u gr = {((__float_as_uint(v0) + 0x80u) & 0xffffff00u) | tlo, thi, ((__float_as_uint(v1) + 0x80u) & 0xffffff00u) | tlo, thi};
        __builtin_amdgcn_raw_buffer_store_b128(gr, rsW, WS_P + (h * 8 + sub) * 4096 + part * 2048 + (lane + 64 * u) * 16, 0, AUX_SC1);
      }
    }
  };
  const unsigned pq_off = WS_P + (h * 8 + w) * 4096 + b * 1024 + lane * 16;
  v4u QG;
  static_for<PF>([&](auto I) {
    constexpr int i = I.v;
    pace(I);
    if constexpr (i == WIN - 1) {
      unsigned elo = (unsigned)epoch, ehi = (unsigned)(epoch >> 32);
      asm volatile("" : "+v"(elo), "+v"(ehi));
      const unsigned long long ep1 = (((unsigned long long)ehi << 32) | elo) + 1ull;
      ep_next = ep1;
      tag32 = (unsigned)ep1; tlo = (unsigned)ep1 & 0xffu; thi = (unsigned)(ep1 >> 8);
      STAMP(8);
      project(w >> 2);
      STAMP(1);
    }
    if constexpr (i == POLL_AT) QG = bld<AUX_SC1>(rsW, pq_off, 0);
    SB();
  });
  auto tagged = [&](const v4u& a) { return ((a.x & 0xffu) ^ tlo) | (a.y ^ thi) | ((a.z & 0xffu) ^ tlo) | (a.w ^ thi); };
  if (!__all(tagged(QG) == 0u)) {
    for (unsigned sp = 0; sp < SPIN_CAP; ++sp) {
      __builtin_amdgcn_s_sleep(POLL_SLEEP);
      MEMFENCE();
      QG = bld<AUX_SC1>(rsW, pq_off, 0);
      if (__all(tagged(QG) == 0u)) break;
    }
  }
  *(float2*)&lds[L_Q + w * 128 + 2 * lane] = make_float2(__uint_as_float(QG.x & 0xffffff00u), __uint_as_float(QG.z & 0xffffff00u));
  __syncthreads();
  t_q = (unsigned)__builtin_amdgcn_s_memrealtime();
  STAMP(7);
  unsigned q0, q1, q2, q3;
  {
    float qv[8];
    const f32x4* p0 = (const f32x4*)&lds[L_Q + (2 * g) * 128 + 8 * c];
    const f32x4* p1 = (const f32x4*)&lds[L_Q + (2 * g + 1) * 128 + 8 * c];
    const f32x4 a0 = p0[0], a1 = p0[1], b0 = p1[0], b1 = p1[1];
#pragma unroll
    for (int e = 0; e < 4; ++e) { qv[e] = xrow16_sum(a0[e] + b0[e]); qv[4 + e] = xrow16_sum(a1[e] + b1[e]); }
    q0 = cvtpk(qv[0], qv[1]); q1 = cvtpk(qv[2], qv[3]); q2 = cvtpk(qv[4], qv[5]); q3 = cvtpk(qv[6], qv[7]);
  }
  SB();

  const int n0 = has_ffn ? 64 * h + 16 * s : 0;
  const rsrc_t rD = mk_rsrc((const char*)wd + (size_t)n0 * 1280, has_ffn ? 16 * 1280 : 0);
  const rsrc_t rO = mk_rsrc((const char*)wo + (size_t)(h * 128 + 16 * w) * 1280, 16 * 1280);
  const unsigned ovoff = g * 1280 + c * 16;
  const unsigned dvoff = g < 2 ? (2 * w + g) * 1280 + c * 16 : OOB;
  v4u DW[5], OW[20];
  f32x4 ya[5];
  unsigned GX, GW;
  v4u FG0, FG1;
  const unsigned pf_off = w == 7 ? WS_P + (h * 8 + g) * 4096 + 2048 + sub * 256 + c * 16 : OOB;
  constexpr int OW_K0 = OWK0, OW_STR = OWSTR, OW_NK = (64 - OW_K0 + OW_STR - 1) / OW_STR;

  float sc[4];
  float x1[2], x2[2], x3[2], x4[2];
  auto kcomp = [&](auto I) {
    constexpr int i = I.v;
    if constexpr (i == 26) {
      const int e = 40 * w + (lane < 40 ? lane : 0);
      GX = ((const GAS unsigned*)((const char*)x + b * 1280))[e];
      GW = ((const GAS unsigned*)gw)[e];
      __builtin_amdgcn_raw_buffer_store_b128(v4u{SENT, SENT, SENT, SENT}, rsW, tid < 160 ? WS_Y + yrow * 2560 + tid * 16 : OOB, 0, AUX_SC1);
      FG0 = bld<AUX_SC1>(rsW, pf_off, 0);
      FG1 = bld<AUX_SC1>(rsW, pf_off + (w == 7 ? 4 * 4096 : 0), 0);
    }
    if constexpr (i == 36) {
      float gp = bfround(bf2f(GX & 0xffffu) * bf2f(GW & 0xffffu)) + bfround(__uint_as_float(GX & 0xffff0000u) * __uint_as_float(GW & 0xffff0000u));
      gp = wave_sum(lane < 40 ? gp : 0.f);
      lds[L_GP + w] = gp;
      if (w == 7) {
        if (!__all((tagged(FG0) | tagged(FG1)) == 0u)) {
          for (unsigned sp = 0; sp < SPIN_CAP; ++sp) {
            __builtin_amdgcn_s_sleep(POLL_SLEEP);
            MEMFENCE();
            FG0 = bld<AUX_SC1>(rsW, pf_off, 0); FG1 = bld<AUX_SC1>(rsW, pf_off + 4 * 4096, 0);
            if (__all((tagged(FG0) | tagged(FG1)) == 0u)) break;
          }
        }
        const float s0 = xrow16_sum(__uint_as_float(FG0.x & 0xffffff00u) + __uint_as_float(FG1.x & 0xffffff00u));
        const float s1 = xrow16_sum(__uint_as_float(FG0.z & 0xffffff00u) + __uint_as_float(FG1.z & 0xffffff00u));
        unsigned hb[2];
#pragma unroll
        for (int u2 = 0; u2 < 2; ++u2) {
          const float sv = u2 ? s1 : s0;
          const float gt = bfround(sv), up = bfround(dpp<XOR8>(sv));
          const float u = 0.7978845608028654f * (gt + 0.044715f * gt * gt * gt);
          const float th = 1.f - 2.f / (1.f + __expf(2.f * u));
          const float ge = bfround(0.5f * gt * (1.f + th));
          hb[u2] = cvtpk(ge * up, 0.f);
        }
        if (lane < 8) *(uint2*)&((unsigned*)lds)[L_H + 2 * lane] = make_uint2(hb[0], hb[1]);
      }
    }
    if constexpr (i >= 40 && i < 50 && (i & 1) == 0) DW[(i - 40) / 2] = bld<WNT>(rD, dvoff, ((i - 40) / 2) * 256);
    if constexpr (OW_K0 >= 0 && i >= OW_K0 && (i - OW_K0) % OW_STR == 0) {
      constexpr int f = (i - OW_K0) / OW_STR;
      OW[f] = bld<OWAUX>(rO, ovoff + (f % 5) * 256, (f / 5) * 5120);
    }
    const v4u cur = st[i];
    float d = dot2(cur.x, q0, 0.f);
    d = dot2(cur.y, q1, d); d = dot2(cur.z, q2, d); d = dot2(cur.w, q3, d);
#define MERGE(A, B, CTRL, BIT) merge2<CTRL>(A, B, (c & (BIT)) != 0)
    x1[i & 1] = d;
    if constexpr (i & 1) {
      x2[(i >> 1) & 1] = MERGE(x1[0], x1[1], XOR8, 8);
      if constexpr ((i >> 1) & 1) {
        x3[(i >> 2) & 1] = MERGE(x2[0], x2[1], XOR7, 4);
        if constexpr ((i >> 2) & 1) {
          x4[(i >> 3) & 1] = MERGE(x3[0], x3[1], XOR2, 2);
          if constexpr ((i >> 3) & 1) sc[i / 16] = MERGE(x4[0], x4[1], XOR1, 1);
          if constexpr (!((i >> 3) & 1)) asm volatile("" : "+v"(x4[0]));
        } else asm volatile("" : "+v"(x3[0]));
      } else asm volatile("" : "+v"(x2[0]));
    } else asm volatile("" : "+v"(x1[0]));
#undef MERGE
    if constexpr (i % 16 == 15) asm volatile("" : "+v"(sc[i / 16]));
  };
  static_for<PF>([&](auto T) {
    constexpr int t = T.v;
    pace(IC<PF + t>{});
    kcomp(IC<2 * t>{});
    kcomp(IC<2 * t + 1>{});
    SB();
  });
  static_for<64 - 2 * PF>([&](auto T) {
    constexpr int i = 2 * PF + T.v;
    pace(IC<i>{});
    kcomp(IC<i>{});
    SB();
  });
  STAMP(2);
  constexpr float SCALE = 0.08838834764831845f, LOG2E = 1.4426950408889634f;
  const unsigned tag = tag32;
  unsigned PP[4];
  float mC;
  {
#pragma unroll
    for (int t = 0; t < 4; ++t) sc[t] = bfround(bfround(sc[t]) * SCALE);
    const float m = wave_max(fmaxf(fmaxf(sc[0], sc[1]), fmaxf(sc[2], sc[3])));
    mC = m * LOG2E;
    float l = 0.f;
#pragma unroll
    for (int t = 0; t < 4; ++t) {
      const float p = __builtin_amdgcn_exp2f((sc[t] - m) * LOG2E);
      l += p;
      PP[t] = split_hilo(p);
    }
    l = wave_sum(l);
    if (lane == 0) { lds[L_MW + w] = mC; lds[L_LW + w] = l; }
    LGKM0();
    unsigned old = 0;
    if (lane == 0) old = __hip_atomic_fetch_add((LAS unsigned*)lds + L_CNT + 1, 1u, __ATOMIC_RELAXED, __HIP_MEMORY_SCOPE_WORKGROUP);
    old = __builtin_amdgcn_readfirstlane(old);
    MEMFENCE();
    if (old == NWAVE - 1) {
      const f32x4 m0 = *(const f32x4*)&lds[L_MW], m1 = *(const f32x4*)&lds[L_MW + 4];
      const f32x4 l0 = *(const f32x4*)&lds[L_LW], l1 = *(const f32x4*)&lds[L_LW + 4];
      const float M = fmaxf(fmaxf(fmaxf(m0[0], m0[1]), fmaxf(m0[2], m0[3])), fmaxf(fmaxf(m1[0], m1[1]), fmaxf(m1[2], m1[3])));
      float L = 0.f;
#pragma unroll
      for (int k = 0; k < 4; ++k) L += l0[k] * __builtin_amdgcn_exp2f(m0[k] - M) + l1[k] * __builtin_amdgcn_exp2f(m1[k] - M);
      const float lse = M + __builtin_amdgcn_logf(L);
      if (lane == 0)
        __hip_atomic_store((GAS unsigned long long*)(ws + WS_G) + yrow, ((unsigned long long)tag << 32) | __float_as_uint(lse), __ATOMIC_RELAXED, __HIP_MEMORY_SCOPE_AGENT);
    }
  }
  const Sel selM = make_sel(c, c >= 8);

  f32x4 acc = {0.f, 0.f, 0.f, 0.f};
  float gate = 0.f;
  v4u GR = {0, 0, 0, 0}, GR2 = {0, 0, 0, 0};
  float fac = 0.f;
  const bool gw6 = w == 6, red = bid < NRED;
  const unsigned g_off = gw6 && (b == (bid & 1) && red ? true : (lane >> 1) == h) ? WS_G + b * 1024 + lane * 16 : OOB;
  const unsigned g_off2 = gw6 && red && (b != (bid & 1) || bid == 0) ? WS_G + (bid == 0 ? 1 : (bid & 1)) * 1024 + lane * 16 : OOB;
  auto vstep = [&](auto I) {
    constexpr int i = I.v;
    pace(IC<64 + i>{});
    if constexpr (64 + i >= OW_K0 && (64 + i - OW_K0) % OW_STR == 0 && (64 + i - OW_K0) / OW_STR < 20) {
      constexpr int f = (64 + i - OW_K0) / OW_STR;
      OW[f] = bld<OWAUX>(rO, ovoff + (f % 5) * 256, (f / 5) * 5120);
    }
    if constexpr (i == 36) { GR = bld<AUX_SC1>(rsW, g_off, 0); GR2 = bld<AUX_SC1>(rsW, g_off2, 0); }
    constexpr int i4 = i % 16, src = ((i4 & 1) << 3) | ((i4 & 2) << 1) | ((i4 & 4) >> 1) | ((i4 & 8) >> 3);
    const unsigned pb = bcast16<src>(PP[i / 16]);
    acc = mfma(st[64 + i], sel_frag(pb, selM), acc);
    if constexpr (i == 27 || i == 28 || i == 43 || i == 44 || i == 55 || i == 56) asm volatile("" : "+v"(acc));
    if constexpr (i == 28) {
      for (unsigned sp = 0; sp < SPIN_CAP; ++sp) {
        const unsigned cnt = __hip_atomic_load((LAS unsigned*)lds + L_CNT + 1, __ATOMIC_RELAXED, __HIP_MEMORY_SCOPE_WORKGROUP);
        if (__builtin_amdgcn_readfirstlane(cnt) >= NWAVE) break;
        __builtin_amdgcn_s_sleep(1);
      }
      MEMFENCE();
      const f32x4 g0 = *(const f32x4*)&lds[L_GP], g1 = *(const f32x4*)&lds[L_GP + 4];
      const float z = bfround(((g0[0] + g0[1]) + (g0[2] + g0[3])) + ((g1[0] + g1[1]) + (g1[2] + g1[3])));
      gate = bfround(1.f / (1.f + __expf(-z)));
      {
        const unsigned hv = ((const unsigned*)lds)[L_H + 2 * w + (g & 1)];
        const v4u bh = sel_frag(hv, selLo);
        const float omg = has_ffn ? bfround(1.f - gate) : 0.f;
#pragma unroll
        for (int k = 0; k < 5; ++k) {
          const f32x4 yf = mfma(DW[k], bh, f32x4{0.f, 0.f, 0.f, 0.f});
          ya[k] = omg * yf;
          asm volatile("" : "+v"(ya[k]));
        }
      }
    }
    if constexpr (i == 44) {
      if (gw6) {
        auto bad = [&]() { return (g_off != OOB && ((GR.y ^ tag) | (GR.w ^ tag)) != 0u) || (g_off2 != OOB && ((GR2.y ^ tag) | (GR2.w ^ tag)) != 0u); };
        if (__any(bad())) {
          for (unsigned sp = 0; sp < SPIN_CAP; ++sp) {
            __builtin_amdgcn_s_sleep(8);
            MEMFENCE();
            GR = bld<AUX_SC1>(rsW, g_off, 0); GR2 = bld<AUX_SC1>(rsW, g_off2, 0);
            if (!__any(bad())) break;
          }
        }
        const float e0 = __uint_as_float(__builtin_amdgcn_readlane(GR.x, 2 * h)), e1 = __uint_as_float(__builtin_amdgcn_readlane(GR.z, 2 * h));
        const float e2 = __uint_as_float(__builtin_amdgcn_readlane(GR.x, 2 * h + 1)), e3 = __uint_as_float(__builtin_amdgcn_readlane(GR.z, 2 * h + 1));
        const float M = fmaxf(fmaxf(e0, e1), fmaxf(e2, e3));
        const float S = (__builtin_amdgcn_exp2f(e0 - M) + __builtin_amdgcn_exp2f(e1 - M)) + (__builtin_amdgcn_exp2f(e2 - M) + __builtin_amdgcn_exp2f(e3 - M));
        if (lane == 0) { lds[L_LSE] = M; lds[L_LSE + 1] = S; }
        LGKM0();
        if (lane == 0) __hip_atomic_store((LAS unsigned*)lds + L_CNT + 4, 1u, __ATOMIC_RELAXED, __HIP_MEMORY_SCOPE_WORKGROUP);
        if (bid == 0 && lane == 0)
          __hip_atomic_store((GAS unsigned long long*)(ws + WS_EPOCH), ep_next, __ATOMIC_RELAXED, __HIP_MEMORY_SCOPE_AGENT);
      }
    }
    if constexpr (i == 56) {
      for (unsigned sp = 0; sp < SPIN_CAP; ++sp) {
        const unsigned f = __hip_atomic_load((LAS unsigned*)lds + L_CNT + 4, __ATOMIC_RELAXED, __HIP_MEMORY_SCOPE_WORKGROUP);
        if (__builtin_amdgcn_readfirstlane(f) != 0u) break;
        __builtin_amdgcn_s_sleep(1);
      }
      MEMFENCE();
      fac = gate * __builtin_amdgcn_exp2f(mC - lds[L_LSE]) / lds[L_LSE + 1];
    }
    SB();
  };
  static_for<64>(vstep);
  STAMP(3);
#pragma unroll
  for (int r = 0; r < 4; ++r) acc[r] = (acc[r] + dpp<XOR8>(acc[r])) * fac;
  if (c < 8) {
#pragma unroll
    for (int r = 0; r < 4; ++r) lds[L_O + (32 * g + 8 * r + c) * 8 + w] = acc[r];
  }
  __syncthreads();
  STAMP(4);
  {
    f32x4 oa[4], ob[4];
#pragma unroll
    for (int i = 0; i < 4; ++i) { const f32x4* op = (const f32x4*)&lds[L_O + (16 * w + 4 * i + g) * 8]; oa[i] = op[0]; ob[i] = op[1]; }
    SB();
#pragma unroll
    for (int i = 0; i < 4; ++i) {
      const float o = ((oa[i][0] + oa[i][1]) + (oa[i][2] + oa[i][3])) + ((ob[i][0] + ob[i][1]) + (ob[i][2] + ob[i][3]));
      const v4u bf = sel_frag(cvtpk(o, o), selLo);
#pragma unroll
      for (int k = 0; k < 5; ++k) ya[k] = mfma(OW[5 * i + k], bf, ya[k]);
    }
  }
  if (c < 8) {
#pragma unroll
    for (int k = 0; k < 5; ++k) *(f32x4*)&lds[L_Y + w * 640 + k * 128 + (g * 8 + c) * 4] = ya[k];
  }
  __syncthreads();
  if (tid < 160) {
    f32x4 t[8];
#pragma unroll
    for (int ww = 0; ww < 8; ++ww) t[ww] = *(const f32x4*)&lds[L_Y + ww * 640 + 4 * tid];
    const f32x4 tt = ((t[0] + t[1]) + (t[2] + t[3])) + ((t[4] + t[5]) + (t[6] + t[7]));
    v4u u = __builtin_bit_cast(v4u, tt);
#pragma unroll
    for (int e = 0; e < 4; ++e) u[e] = (u[e] == SENT) ? 0x7fc00000u : u[e];
    __builtin_amdgcn_raw_buffer_store_b128(u, rsW, WS_Y + yrow * 2560 + tid * 16, 0, AUX_SC1);
  }
  STAMP(5);
  if (bid < NRED) {
    const int rb = bid & 1, cb = bid >> 1;
    const unsigned off0 = WS_Y + (rb * 128 + 16 * w + (lane >> 3)) * 2560 + cb * 128 + (lane & 7) * 16;
    v4u r0 = {SENT, SENT, SENT, SENT}, r1 = {SENT, SENT, SENT, SENT};
    for (unsigned sp = 0; sp < SPIN_CAP; ++sp) {
      MEMFENCE();
      const bool n0_ = (r0.x == SENT) | (r0.y == SENT) | (r0.z == SENT) | (r0.w == SENT), n1_ = (r1.x == SENT) | (r1.y == SENT) | (r1.z == SENT) | (r1.w == SENT);
      const v4u t0 = bld<AUX_SC1>(rsW, n0_ ? off0 : OOB, 0), t1 = bld<AUX_SC1>(rsW, n1_ ? off0 + 8 * 2560 : OOB, 0);
      if (n0_) r0 = t0;
      if (n1_) r1 = t1;
      const bool ok = (r0.x != SENT) & (r0.y != SENT) & (r0.z != SENT) & (r0.w != SENT) & (r1.x != SENT) & (r1.y != SENT) & (r1.z != SENT) & (r1.w != SENT);
      if (__all(ok)) break;
      __builtin_amdgcn_s_sleep(FIN_SLEEP);
    }
    f32x4 t = __builtin_bit_cast(f32x4, r0) + __builtin_bit_cast(f32x4, r1);
#pragma unroll
    for (int e = 0; e < 4; ++e) { float v = t[e]; v += dpp<XOR8>(v); t[e] = xrow16_sum(v); }
    if (lane < 8) *(f32x4*)&lds[L_R + w * 32 + lane * 4] = t;
    __syncthreads();
    if (tid < 16) {
      const int r = tid >> 2, q2 = (tid & 3) * 2;
      float s0 = 0.f, s1 = 0.f;
#pragma unroll
      for (int ww = 0; ww < 8; ++ww) { s0 += lds[L_R + ww * 32 + q2 * 4 + r]; s1 += lds[L_R + ww * 32 + (q2 + 1) * 4 + r]; }
      ((unsigned*)out)[rb * 320 + cb * 16 + 4 * r + (tid & 3)] = cvtpk(s0, s1);
    }
  }
}

extern "C" void kernel_launch(void* const* d_in, const int* in_sizes, int n_in, void* d_out, int out_size, void* d_ws,
                              size_t ws_size, hipStream_t stream) {
  (void)in_sizes; (void)n_in; (void)out_size; (void)ws_size;
  (void)hipFuncSetAttribute((const void*)fused_decode, hipFuncAttributeMaxDynamicSharedMemorySize, LDS_BYTES);
  fused_decode<<<256, NTHR, LDS_BYTES, stream>>>((const u16*)d_in[0], (const u16*)d_in[1], (const u16*)d_in[2], (const u16*)d_in[3],
                                                 (const u16*)d_in[4], (const u16*)d_in[6], (const u16*)d_in[7], (const u16*)d_in[8],
                                                 (const u16*)d_in[9], (u16*)d_out, (unsigned char*)d_ws);
}
```
